```python
import torch

def _softmax(x, axis=-1):
    return torch.softmax(x, dim=axis)
def _gelu(x):
    return torch.nn.functional.gelu(x, approximate="tanh")
def _silu(x):
    return torch.nn.functional.silu(x)
def _sigmoid(x):
    return torch.sigmoid(x)
def _layernorm(x, axis=-1, eps=1e-5):
    m = x.mean(dim=axis, keepdim=True)
    v = x.var(dim=axis, keepdim=True, unbiased=False)
    return (x - m) / torch.sqrt(v + eps)
def _scatter_add(tgt, idx, src, axis=0):
    return tgt.clone().index_add_(axis, idx, src)
def _segment_sum(src, seg, n):
    out = torch.zeros((n,) + src.shape[1:], dtype=src.dtype)
    return out.index_add_(0, seg, src)
def _pad(x, pw):
    flat = tuple(v for pair in reversed(pw) for v in pair)
    return torch.nn.functional.pad(x, flat)

kernel_name = 'sequential_preLN__sliding_block__glu_gated_swiglu__prompt'

def setup_inputs():

    _w = (lambda a: (lambda t: t.to(torch.bfloat16) if t.is_floating_point() else t)(torch.as_tensor(a)))
    def _A_setup():
        B, S, D = 1, 2048, 1024
        return {
            "x": _w((lambda *s: torch.randn(*s, dtype=torch.bfloat16) * 0.05)(B, S, D)),
            "wqkv": _w((lambda *s: torch.randn(*s, dtype=torch.bfloat16) * 0.05)(D, 3 * D)),
            "wo": _w((lambda *s: torch.randn(*s, dtype=torch.bfloat16) * 0.05)(D, D)),
        }
    _A_in = _A_setup()

    def _B_setup():
        B, S, D, F = 1, 2048, 1024, 3584
        return {
            "x": _w((lambda *s: torch.randn(*s, dtype=torch.bfloat16) * 0.05)(B, S, D)),
            "w1": _w((lambda *s: torch.randn(*s, dtype=torch.bfloat16) * 0.8)(D, F) * (2.0 / D) ** 0.5),
            "w3": _w((lambda *s: torch.randn(*s, dtype=torch.bfloat16) * 0.8)(D, F) * (2.0 / D) ** 0.5),
            "w2": _w((lambda *s: torch.randn(*s, dtype=torch.bfloat16) * 0.8)(F, D) * (2.0 / F) ** 0.5),
        }
    _B_in = _B_setup()

    return {
        "A_x": _A_in["x"],
        "A_wqkv": _A_in["wqkv"],
        "A_wo": _A_in["wo"],
        "B_x": _B_in["x"],
        "B_w1": _B_in["w1"],
        "B_w3": _B_in["w3"],
        "B_w2": _B_in["w2"],
    }

def reference(A_x, A_wqkv, A_wo, B_x, B_w1, B_w3, B_w2):
    def _A_ref(x, wqkv, wo):
        B, S, D = x.shape
        H = 32; Dh = D // H; W = 256
        qkv = (lambda x, *d: x.permute(*d))((x @ wqkv).reshape(B, S, 3, H, Dh), 2, 0, 3, 1, 4)
        q, k, v = qkv[0], qkv[1], qkv[2]
        scores = torch.einsum("bhqd,bhkd->bhqk", q, k) * (Dh ** -0.5)
        idx = torch.arange(S)
        mask = (idx[:, None] - idx[None, :] >= 0) & (idx[:, None] - idx[None, :] < W)
        scores = torch.where(mask[None, None], scores, -1e9)
        p = _softmax(scores, axis=-1)
        o = torch.einsum("bhqk,bhkd->bhqd", p, v)
        return (lambda x, *d: x.permute(*d))(o, 0, 2, 1, 3).reshape(B, S, D) @ wo

    def _B_ref(x, w1, w3, w2):
        gate = torch.einsum("bsd,df->bsf", x, w1)
        up = torch.einsum("bsd,df->bsf", x, w3)
        h = _silu(gate) * up
        out = torch.einsum("bsf,fd->bsd", h, w2)
        return out

    x = A_x
    h = x + _A_ref(_layernorm(x, axis=-1), A_wqkv, A_wo)
    h2 = h + _B_ref(_layernorm(h, axis=-1), B_w1, B_w3, B_w2)
    return h2
```

```cpp
#include <hip/hip_runtime.h>
#include <hip/hip_bf16.h>
#include <stdint.h>

typedef unsigned short bf16_t;
typedef short bf16x8 __attribute__((ext_vector_type(8)));
typedef short s16x4 __attribute__((ext_vector_type(4)));
typedef float f32x4 __attribute__((ext_vector_type(4)));
typedef unsigned u32x4 __attribute__((ext_vector_type(4)));
typedef unsigned u32x2 __attribute__((ext_vector_type(2)));
#define LAS __attribute__((address_space(3)))
#define DI __device__ __forceinline__

constexpr int S_ = 2048, D_ = 1024, F_ = 3584, H_ = 32, DH_ = 32, WIN_ = 256;

DI float bf2f(unsigned short b) { return __uint_as_float(((unsigned)b) << 16); }
DI unsigned pk_bf16(float lo, float hi) {
    typedef __bf16 bf2 __attribute__((ext_vector_type(2)));
    bf2 v; v.x = (__bf16)lo; v.y = (__bf16)hi;
    return __builtin_bit_cast(unsigned, v);
}
DI s16x4 tr_read(LAS unsigned char* p) { return __builtin_amdgcn_ds_read_tr16_b64_v4i16((LAS s16x4*)p); }
DI void glds16(const void* g, LAS unsigned char* l) {
    __builtin_amdgcn_global_load_lds((const __attribute__((address_space(1))) unsigned*)g, (LAS unsigned*)l, 16, 0, 0);
}
#define WAIT_V(n) asm volatile("s_waitcnt vmcnt(" #n ")" ::: "memory")
#define WAIT_L(n) asm volatile("s_waitcnt lgkmcnt(" #n ")" ::: "memory")
#define BAR __builtin_amdgcn_s_barrier()
#define SCHED __builtin_amdgcn_sched_barrier(0)

__global__ __launch_bounds__(256) void ln_kernel(const bf16_t* __restrict__ in, bf16_t* __restrict__ out) {
    const int row = blockIdx.x * 4 + (threadIdx.x >> 6), lane = threadIdx.x & 63;
    const u32x4* p = (const u32x4*)(in + (size_t)row * D_);
    u32x4 a = p[lane], b = p[lane + 64];
    float v[16];
#pragma unroll
    for (int i = 0; i < 4; ++i) { v[2 * i] = __uint_as_float(a[i] << 16); v[2 * i + 1] = __uint_as_float(a[i] & 0xffff0000u);
                                  v[8 + 2 * i] = __uint_as_float(b[i] << 16); v[8 + 2 * i + 1] = __uint_as_float(b[i] & 0xffff0000u); }
    float s = 0.f;
#pragma unroll
    for (int i = 0; i < 16; ++i) s += v[i];
#pragma unroll
    for (int o = 32; o >= 1; o >>= 1) s += __shfl_xor(s, o);
    const float mean = s * (1.0f / D_);
    float q = 0.f;
#pragma unroll
    for (int i = 0; i < 16; ++i) { v[i] -= mean; q += v[i] * v[i]; }
#pragma unroll
    for (int o = 32; o >= 1; o >>= 1) q += __shfl_xor(q, o);
    const float rstd = 1.0f / sqrtf(q * (1.0f / D_) + 1e-5f);
    u32x4 oa, ob;
#pragma unroll
    for (int i = 0; i < 4; ++i) { oa[i] = pk_bf16(v[2 * i] * rstd, v[2 * i + 1] * rstd); ob[i] = pk_bf16(v[8 + 2 * i] * rstd, v[8 + 2 * i + 1] * rstd); }
    u32x4* po = (u32x4*)(out + (size_t)row * D_);
    po[lane] = oa; po[lane + 64] = ob;
}

DI int bx_of_k(int k) { return ((k >> 1) & 1) | (((k >> 3) & 1) << 1); }

enum { EPI_QKV = 0, EPI_RES = 1 };

template <int BM, int BN, int WM, int WN, int NBUF, int EPI, int RM, int RN>
__global__ __launch_bounds__(512, 2) void gemm_pp(const bf16_t* __restrict__ A, int lda, const bf16_t* __restrict__ W, int ldb, int K,
                                                   const bf16_t* __restrict__ res, bf16_t* __restrict__ out, int tiles_m) {
    extern __shared__ __attribute__((aligned(16))) unsigned char smem_raw[];
    LAS unsigned char* lds = (LAS unsigned char*)smem_raw;
    constexpr int MT = BM / WM / 16, NT = BN / WN / 16;
    constexpr int NPA = BM / 64, NPB = BN / 64, NP = NPA + NPB;
    constexpr int ABYTES = BM * 128, TILE_BYTES = (BM + BN) * 128;
    static_assert(WM * WN == 8, "8 waves");
    const int tid = threadIdx.x, wid = __builtin_amdgcn_readfirstlane(tid >> 6), lane = tid & 63;
    const int grp = wid >> 2, wm = wid / WN, wn = wid % WN, fr = lane & 15, fq = lane >> 4;
    int tm, tn;
    { const int bid = blockIdx.x, xcd = bid & 7, idx = bid >> 3; const int regm = tiles_m / RM;
      const int rr = xcd % regm, rc = xcd / regm; tm = rr * RM + (idx % RM); tn = rc * RN + (idx / RM); }
    const int m0 = tm * BM, n0 = tn * BN;
    const int nt = K / 64;
    const bf16_t* asrc[NPA]; const bf16_t* bsrc[NPB];
#pragma unroll
    for (int i = 0; i < NPA; ++i) { const int row = (wid + 8 * i) * 8 + (lane >> 3), ch = (lane & 7) ^ ((row >> 1) & 7);
        asrc[i] = A + (size_t)(m0 + row) * lda + ch * 8; }
#pragma unroll
    for (int j = 0; j < NPB; ++j) { const int k = wid * 8 + (lane >> 3), s16 = lane & 7, n16 = (s16 >> 1) ^ bx_of_k(k);
        bsrc[j] = W + (size_t)k * ldb + n0 + j * 64 + n16 * 16 + (s16 & 1) * 8; }
    const size_t bstep = (size_t)64 * ldb;
    auto stage = [&](int t, int buf) {
        LAS unsigned char* base = lds + buf * TILE_BYTES + wid * 1024;
#pragma unroll
        for (int i = 0; i < NPA; ++i) glds16(asrc[i] + (size_t)t * 64, base + i * 8192);
#pragma unroll
        for (int j = 0; j < NPB; ++j) glds16(bsrc[j] + (size_t)t * bstep, base + ABYTES + j * 8192);
    };
    const int sw = (fr >> 1) & 7;
    const int aoff0 = (wm * MT * 16 + fr) * 128 + ((fq ^ sw) * 16), aoff1 = aoff0 ^ 64;
    int boff[NT];
    { const int q = (lane & 15) >> 2, p = lane & 3, xl = ((q >> 1) & 1) | ((fq & 1) << 1);
#pragma unroll
      for (int n = 0; n < NT; ++n) { const int ntg = wn * NT + n; boff[n] = ABYTES + (ntg >> 2) * 8192 + (8 * fq + q) * 128 + (((ntg & 3) ^ xl) * 32) + p * 8; } }
    f32x4 acc[MT][NT];
#pragma unroll
    for (int m = 0; m < MT; ++m)
#pragma unroll
        for (int n = 0; n < NT; ++n) acc[m][n] = (f32x4){0.f, 0.f, 0.f, 0.f};
#pragma unroll
    for (int t = 0; t < NBUF - 1; ++t) stage(t, t);
    if constexpr ((NBUF - 2) * NP == 5) WAIT_V(5); else if constexpr ((NBUF - 2) * NP == 10) WAIT_V(10);
    else if constexpr ((NBUF - 2) * NP == 3) WAIT_V(3); else if constexpr ((NBUF - 2) * NP == 6) WAIT_V(6);
    else if constexpr ((NBUF - 2) * NP == 9) WAIT_V(9); else if constexpr ((NBUF - 2) * NP == 12) WAIT_V(12); else WAIT_V(0);
    BAR;
    if (grp == 1) BAR;
    int buf = 0, pbuf = NBUF - 1;
    for (int t = 0; t < nt; ++t) {
        LAS unsigned char* cb = lds + buf * TILE_BYTES;
        bf16x8 af[MT][2], bfr[NT][2];
#pragma unroll
        for (int n = 0; n < NT; ++n)
#pragma unroll
            for (int kk = 0; kk < 2; ++kk) { const s16x4 lo = tr_read(cb + boff[n] + kk * 4096), hi = tr_read(cb + boff[n] + kk * 4096 + 512);
                bfr[n][kk] = __builtin_shufflevector(lo, hi, 0, 1, 2, 3, 4, 5, 6, 7); }
#pragma unroll
        for (int m = 0; m < MT; ++m) { af[m][0] = *(const LAS bf16x8*)(cb + aoff0 + m * 2048); af[m][1] = *(const LAS bf16x8*)(cb + aoff1 + m * 2048); }
        if (t + NBUF - 1 < nt) {
            stage(t + NBUF - 1, pbuf);
            if constexpr ((NBUF - 2) * NP == 5) WAIT_V(5); else if constexpr ((NBUF - 2) * NP == 10) WAIT_V(10);
            else if constexpr ((NBUF - 2) * NP == 3) WAIT_V(3); else if constexpr ((NBUF - 2) * NP == 6) WAIT_V(6);
            else if constexpr ((NBUF - 2) * NP == 9) WAIT_V(9); else if constexpr ((NBUF - 2) * NP == 12) WAIT_V(12); else WAIT_V(0);
        } else { WAIT_V(0); }
        WAIT_L(0);
        BAR;
        __builtin_amdgcn_s_setprio(1);
#pragma unroll
        for (int kk = 0; kk < 2; ++kk)
#pragma unroll
            for (int m = 0; m < MT; ++m)
#pragma unroll
                for (int n = 0; n < NT; ++n) acc[m][n] = __builtin_amdgcn_mfma_f32_16x16x32_bf16(bfr[n][kk], af[m][kk], acc[m][n], 0, 0, 0);
        __builtin_amdgcn_s_setprio(0);
        BAR;
        SCHED;
        buf = (buf + 1 == NBUF) ? 0 : buf + 1; pbuf = (pbuf + 1 == NBUF) ? 0 : pbuf + 1;
    }
    if (grp == 0) BAR;
#pragma unroll
    for (int m = 0; m < MT; ++m) {
        const int row = m0 + (wm * MT + m) * 16 + fr;
#pragma unroll
        for (int n = 0; n < NT; ++n) {
            const int col = n0 + (wn * NT + n) * 16 + 4 * fq;
            f32x4 v = acc[m][n];
            if constexpr (EPI == EPI_QKV) {
                u32x2 w; w.x = pk_bf16(v[0], v[1]); w.y = pk_bf16(v[2], v[3]);
                *(u32x2*)(out + ((size_t)(col >> 5) * S_ + row) * 32 + (col & 31)) = w;
            } else {
                const u32x2 r = *(const u32x2*)(res + (size_t)row * D_ + col);
                v[0] += __uint_as_float(r.x << 16); v[1] += __uint_as_float(r.x & 0xffff0000u);
                v[2] += __uint_as_float(r.y << 16); v[3] += __uint_as_float(r.y & 0xffff0000u);
                u32x2 w; w.x = pk_bf16(v[0], v[1]); w.y = pk_bf16(v[2], v[3]);
                *(u32x2*)(out + (size_t)row * D_ + col) = w;
            }
        }
    }
}

__global__ __launch_bounds__(512, 2) void gemm_gateup(const bf16_t* __restrict__ A, const bf16_t* __restrict__ W1, const bf16_t* __restrict__ W3,
                                                       bf16_t* __restrict__ act) {
    extern __shared__ __attribute__((aligned(16))) unsigned char smem_raw[];
    LAS unsigned char* lds = (LAS unsigned char*)smem_raw;
    constexpr int K = D_, HTB = 16384;
    const int tid = threadIdx.x, wid = __builtin_amdgcn_readfirstlane(tid >> 6), lane = tid & 63;
    const int wr = wid >> 2, wc = wid & 3, fr = lane & 15, fq = lane >> 4;
    int tm, tn;
    { const int bid = blockIdx.x, xcd = bid & 7, idx = bid >> 3; tm = (xcd & 1) * 4 + (idx & 3); tn = (xcd >> 1) * 7 + (idx >> 2); }
    const int m0 = tm * 256, f0 = tn * 128;
    constexpr int nt = K / 64;
    unsigned aoffs[2], boffs[2];
#pragma unroll
    for (int i = 0; i < 2; ++i) { const int row = (wid + 8 * i) * 8 + (lane >> 3), ch = (lane & 7) ^ ((row >> 1) & 7);
        aoffs[i] = (unsigned)(row * K + ch * 8) * 2u;
        const int k = wid * 8 + (lane >> 3), s16 = lane & 7, n16 = (s16 >> 1) ^ bx_of_k(k);
        boffs[i] = (unsigned)(k * F_ + i * 64 + n16 * 16 + (s16 & 1) * 8) * 2u; }
    const char* gA0 = (const char*)(A + (size_t)m0 * K); const char* gA1 = gA0 + (size_t)128 * K * 2;
    const char* gB0 = (const char*)(W1 + f0); const char* gB1 = (const char*)(W3 + f0);
    constexpr size_t akstep = 128, bkstep = (size_t)64 * F_ * 2;
    const unsigned ldsw = (unsigned)wid * 1024u;
#define G_SA(b, h) (((b) * 2 + (h)) * HTB)
#define G_SB(b, h) ((4 + (b) * 2 + (h)) * HTB)
#define G_STAGE(bufoff, gbase, voff) do { _Pragma("unroll") for (int _i = 0; _i < 2; ++_i) glds16((gbase) + (voff)[_i], lds + (bufoff) + ldsw + _i * 8192); } while (0)
    const int sw = (fr >> 1) & 7;
    const int aoff0 = (wr * 64 + fr) * 128 + ((fq ^ sw) * 16), aoff1 = aoff0 ^ 64;
    int boff[2];
    { const int q = (lane & 15) >> 2, p = lane & 3, xl = ((q >> 1) & 1) | ((fq & 1) << 1);
#pragma unroll
      for (int n = 0; n < 2; ++n) { const int ntg = wc * 2 + n; boff[n] = (ntg >> 2) * 8192 + (8 * fq + q) * 128 + (((ntg & 3) ^ xl) * 32) + p * 8; } }
#define G_LDA(dst, b, h) do { _Pragma("unroll") for (int m = 0; m < 4; ++m) { dst[m][0] = *(const LAS bf16x8*)(lds + G_SA(b, h) + aoff0 + m * 2048); \
        dst[m][1] = *(const LAS bf16x8*)(lds + G_SA(b, h) + aoff1 + m * 2048); } } while (0)
#define G_LDB(dst, b, h) do { _Pragma("unroll") for (int n = 0; n < 2; ++n) _Pragma("unroll") for (int k = 0; k < 2; ++k) { \
        const s16x4 lo = tr_read(lds + G_SB(b, h) + boff[n] + k * 4096), hi = tr_read(lds + G_SB(b, h) + boff[n] + k * 4096 + 512); \
        dst[n][k] = __builtin_shufflevector(lo, hi, 0, 1, 2, 3, 4, 5, 6, 7); } } while (0)
#define G_MMA(ai, bj, At, Bt) do { __builtin_amdgcn_s_setprio(1); _Pragma("unroll") for (int m = 0; m < 4; ++m) _Pragma("unroll") for (int n = 0; n < 2; ++n) \
        _Pragma("unroll") for (int k = 0; k < 2; ++k) acc[ai][bj][m][n] = __builtin_amdgcn_mfma_f32_16x16x32_bf16(Bt[n][k], At[m][k], acc[ai][bj][m][n], 0, 0, 0); \
        __builtin_amdgcn_s_setprio(0); } while (0)
    f32x4 acc[2][2][4][2];
#pragma unroll
    for (int a = 0; a < 2; ++a)
#pragma unroll
        for (int b = 0; b < 2; ++b)
#pragma unroll
            for (int m = 0; m < 4; ++m)
#pragma unroll
                for (int n = 0; n < 2; ++n) acc[a][b][m][n] = (f32x4){0.f, 0.f, 0.f, 0.f};
    bf16x8 At[4][2], B0[2][2], B1[2][2];
    G_STAGE(G_SB(0, 0), gB0, boffs); G_STAGE(G_SA(0, 0), gA0, aoffs); G_STAGE(G_SB(0, 1), gB1, boffs); G_STAGE(G_SA(0, 1), gA1, aoffs);
    if (wr == 1) BAR;
    WAIT_V(4); BAR;
    G_STAGE(G_SB(1, 0), gB0 + bkstep, boffs); G_STAGE(G_SA(1, 0), gA0 + akstep, aoffs); G_STAGE(G_SB(1, 1), gB1 + bkstep, boffs);
    WAIT_V(6); BAR;
    for (int t = 0; t < nt; t += 2) {
        const int t1 = t + 1, t2 = (t + 2 < nt) ? t + 2 : nt - 1, t3 = (t + 3 < nt) ? t + 3 : nt - 1;
        G_LDB(B0, 0, 0); SCHED; G_LDA(At, 0, 0); G_STAGE(G_SA(1, 1), gA1 + t1 * akstep, aoffs);
        WAIT_L(8); BAR; WAIT_L(0); G_MMA(0, 0, At, B0); BAR; SCHED;
        G_LDB(B1, 0, 1); G_STAGE(G_SB(0, 0), gB0 + t2 * bkstep, boffs);
        BAR; WAIT_L(0); G_MMA(0, 1, At, B1); BAR;
        G_LDA(At, 0, 1); G_STAGE(G_SA(0, 0), gA0 + t2 * akstep, aoffs);
        BAR; WAIT_L(0); G_MMA(1, 0, At, B0); BAR; SCHED;
        G_STAGE(G_SB(0, 1), gB1 + t2 * bkstep, boffs);
        WAIT_V(6); BAR; G_MMA(1, 1, At, B1); BAR;
        G_LDB(B0, 1, 0); SCHED; G_LDA(At, 1, 0); G_STAGE(G_SA(0, 1), gA1 + t2 * akstep, aoffs);
        WAIT_L(8); BAR; WAIT_L(0); G_MMA(0, 0, At, B0); BAR; SCHED;
        G_LDB(B1, 1, 1); G_STAGE(G_SB(1, 0), gB0 + t3 * bkstep, boffs);
        BAR; WAIT_L(0); G_MMA(0, 1, At, B1); BAR;
        G_LDA(At, 1, 1); G_STAGE(G_SA(1, 0), gA0 + t3 * akstep, aoffs);
        BAR; WAIT_L(0); G_MMA(1, 0, At, B0); BAR; SCHED;
        G_STAGE(G_SB(1, 1), gB1 + t3 * bkstep, boffs);
        WAIT_V(6); BAR; G_MMA(1, 1, At, B1); BAR;
    }
    WAIT_V(0);
    if (wr == 0) BAR;
#pragma unroll
    for (int ai = 0; ai < 2; ++ai)
#pragma unroll
        for (int m = 0; m < 4; ++m) {
            const int row = m0 + ai * 128 + wr * 64 + m * 16 + fr;
#pragma unroll
            for (int n = 0; n < 2; ++n) {
                const f32x4 g = acc[ai][0][m][n], u = acc[ai][1][m][n];
                float o[4];
#pragma unroll
                for (int j = 0; j < 4; ++j) o[j] = g[j] / (1.0f + __expf(-g[j])) * u[j];
                u32x2 w; w.x = pk_bf16(o[0], o[1]); w.y = pk_bf16(o[2], o[3]);
                *(u32x2*)(act + (size_t)row * F_ + f0 + wc * 32 + n * 16 + 4 * fq) = w;
            }
        }
#undef G_SA
#undef G_SB
#undef G_STAGE
#undef G_LDA
#undef G_LDB
#undef G_MMA
}

constexpr int AT_ROWS = 336;
__global__ __launch_bounds__(256) void attn_kernel(const bf16_t* __restrict__ qkv, bf16_t* __restrict__ o) {
    __shared__ __attribute__((aligned(16))) unsigned char sm[2 * AT_ROWS * 64];
    LAS unsigned char* lk = (LAS unsigned char*)sm; LAS unsigned char* lv = lk + AT_ROWS * 64;
    const int tid = threadIdx.x, wid = __builtin_amdgcn_readfirstlane(tid >> 6), lane = tid & 63, fr = lane & 15, fq = lane >> 4;
    const int head = blockIdx.x & 31, qb = blockIdx.x >> 5, q0 = qb * 64;
    const bf16_t* Q = qkv + (size_t)head * S_ * 32;
    const bf16_t* Kg = qkv + (size_t)(32 + head) * S_ * 32;
    const bf16_t* Vg = qkv + (size_t)(64 + head) * S_ * 32;
    for (int pc = wid; pc < AT_ROWS / 16; pc += 4) {
        const int row = pc * 16 + (lane >> 2), slot = lane & 3;
        int key = q0 - 256 + row; key = key < 0 ? 0 : (key > S_ - 1 ? S_ - 1 : key);
        const int chk = slot ^ ((-(row >> 2)) & 3), chv = slot ^ (((row >> 2) & 1) << 1);
        glds16(Kg + (size_t)key * 32 + chk * 8, lk + pc * 1024);
        glds16(Vg + (size_t)key * 32 + chv * 8, lv + pc * 1024);
    }
    const int qrow = q0 + wid * 16 + fr;
    const bf16x8 qf = *(const bf16x8*)(Q + (size_t)qrow * 32 + fq * 8);
    WAIT_V(0);
    __syncthreads();
    const int kbase = q0 + wid * 16 - 256;
    const int istart = kbase < 0 ? (-kbase) >> 4 : 0;
    f32x4 s[17];
    const int koff = (wid * 16 + fr) * 64 + ((fq ^ ((-(fr >> 2)) & 3)) * 16);
#pragma unroll
    for (int i = 0; i < 17; ++i) {
        const bf16x8 kf = *(const LAS bf16x8*)(lk + koff + i * 1024);
        s[i] = __builtin_amdgcn_mfma_f32_16x16x32_bf16(kf, qf, (f32x4){0.f, 0.f, 0.f, 0.f}, 0, 0, 0);
    }
    const float NEG = -3.0e38f;
#pragma unroll
    for (int r = 0; r < 4; ++r) { if (!(4 * fq + r > fr)) s[0][r] = NEG; if (!(4 * fq + r <= fr)) s[16][r] = NEG; }
#pragma unroll
    for (int i = 0; i < 16; ++i) if (i < istart) s[i] = (f32x4){NEG, NEG, NEG, NEG};
    float mx = NEG;
#pragma unroll
    for (int i = 0; i < 17; ++i) mx = fmaxf(mx, fmaxf(fmaxf(s[i][0], s[i][1]), fmaxf(s[i][2], s[i][3])));
    mx = fmaxf(mx, __shfl_xor(mx, 16)); mx = fmaxf(mx, __shfl_xor(mx, 32));
    const float c = 0.17677669529663687f * 1.4426950408889634f;
    const float mc = mx * c;
    float sum = 0.f;
#pragma unroll
    for (int i = 0; i < 17; ++i)
#pragma unroll
        for (int r = 0; r < 4; ++r) { const float p = __builtin_amdgcn_exp2f(s[i][r] * c - mc); s[i][r] = p; sum += p; }
    sum += __shfl_xor(sum, 16); sum += __shfl_xor(sum, 32);
    f32x4 oacc[2] = {(f32x4){0.f, 0.f, 0.f, 0.f}, (f32x4){0.f, 0.f, 0.f, 0.f}};
    const int q4 = (lane & 15) >> 2, p4 = lane & 3;
    const int voff = (wid * 16 + 4 * fq + q4) * 64 + p4 * 8;
    const int vx = (fq & 1) << 5;
#pragma unroll
    for (int cc = 0; cc < 9; ++cc) {
        u32x4 pw; pw.x = pk_bf16(s[2 * cc][0], s[2 * cc][1]); pw.y = pk_bf16(s[2 * cc][2], s[2 * cc][3]);
        if (cc < 8) { pw.z = pk_bf16(s[2 * cc + 1][0], s[2 * cc + 1][1]); pw.w = pk_bf16(s[2 * cc + 1][2], s[2 * cc + 1][3]); } else { pw.z = 0u; pw.w = 0u; }
        const bf16x8 pf = __builtin_bit_cast(bf16x8, pw);
#pragma unroll
        for (int dt = 0; dt < 2; ++dt) {
            const s16x4 lo = tr_read(lv + voff + cc * 2048 + ((dt * 32) ^ vx)), hi = tr_read(lv + voff + cc * 2048 + 1024 + ((dt * 32) ^ vx));
            const bf16x8 vf = __builtin_shufflevector(lo, hi, 0, 1, 2, 3, 4, 5, 6, 7);
            oacc[dt] = __builtin_amdgcn_mfma_f32_16x16x32_bf16(vf, pf, oacc[dt], 0, 0, 0);
        }
    }
    const float inv = 1.0f / sum;
#pragma unroll
    for (int dt = 0; dt < 2; ++dt) {
        u32x2 w; w.x = pk_bf16(oacc[dt][0] * inv, oacc[dt][1] * inv); w.y = pk_bf16(oacc[dt][2] * inv, oacc[dt][3] * inv);
        *(u32x2*)(o + (size_t)qrow * D_ + head * 32 + dt * 16 + 4 * fq) = w;
    }
}

extern "C" void kernel_launch(void* const* d_in, const int* in_sizes, int n_in, void* d_out, int out_size, void* d_ws, size_t ws_size,
                              hipStream_t stream) {
    const bf16_t* x = (const bf16_t*)d_in[0];
    const bf16_t* wqkv = (const bf16_t*)d_in[1];
    const bf16_t* wo = (const bf16_t*)d_in[2];
    const bf16_t* w1 = (const bf16_t*)d_in[4];
    const bf16_t* w3 = (const bf16_t*)d_in[5];
    const bf16_t* w2 = (const bf16_t*)d_in[6];
    bf16_t* out = (bf16_t*)d_out;
    char* ws = (char*)d_ws;
    bf16_t* xn = (bf16_t*)(ws);
    bf16_t* qkv = (bf16_t*)(ws + (size_t)4 * 1024 * 1024);
    bf16_t* o = (bf16_t*)(ws + (size_t)20 * 1024 * 1024);
    bf16_t* h = (bf16_t*)(ws + (size_t)24 * 1024 * 1024);
    bf16_t* hn = (bf16_t*)(ws + (size_t)28 * 1024 * 1024);
    bf16_t* act = (bf16_t*)(ws + (size_t)32 * 1024 * 1024);

    ln_kernel<<<S_ / 4, 256, 0, stream>>>(x, xn);
    {
        auto kern = gemm_pp<128, 192, 2, 4, 3, EPI_QKV, 8, 4>;
        const int shm = 3 * (128 + 192) * 128;
        hipFuncSetAttribute((const void*)kern, hipFuncAttributeMaxDynamicSharedMemorySize, shm);
        kern<<<256, 512, shm, stream>>>(xn, D_, wqkv, 3 * D_, D_, nullptr, qkv, 16);
    }
    attn_kernel<<<32 * 32, 256, 0, stream>>>(qkv, o);
    {
        auto kern = gemm_pp<128, 64, 4, 2, 5, EPI_RES, 4, 8>;
        const int shm = 5 * (128 + 64) * 128;
        hipFuncSetAttribute((const void*)kern, hipFuncAttributeMaxDynamicSharedMemorySize, shm);
        kern<<<256, 512, shm, stream>>>(o, D_, wo, D_, D_, x, h, 16);
    }
    ln_kernel<<<S_ / 4, 256, 0, stream>>>(h, hn);
    {
        const int shm = 8 * 16384;
        hipFuncSetAttribute((const void*)gemm_gateup, hipFuncAttributeMaxDynamicSharedMemorySize, shm);
        gemm_gateup<<<224, 512, shm, stream>>>(hn, w1, w3, act);
    }
    {
        auto kern = gemm_pp<128, 64, 4, 2, 5, EPI_RES, 4, 8>;
        const int shm = 5 * (128 + 64) * 128;
        hipFuncSetAttribute((const void*)kern, hipFuncAttributeMaxDynamicSharedMemorySize, shm);
        kern<<<256, 512, shm, stream>>>(act, F_, w2, D_, F_, h, out, 16);
    }
}
```

```cpp
#include <hip/hip_runtime.h>
#include <hip/hip_bf16.h>
#include <stdint.h>

typedef unsigned short bf16_t;
typedef short bf16x8 __attribute__((ext_vector_type(8)));
typedef short s16x4 __attribute__((ext_vector_type(4)));
typedef float f32x4 __attribute__((ext_vector_type(4)));
typedef unsigned u32x4 __attribute__((ext_vector_type(4)));
typedef unsigned u32x2 __attribute__((ext_vector_type(2)));
#define LAS __attribute__((address_space(3)))
#define DI __device__ __forceinline__

constexpr int S_ = 2048, D_ = 1024, F_ = 3584, H_ = 32, DH_ = 32, WIN_ = 256;

DI float bf2f(unsigned short b) { return __uint_as_float(((unsigned)b) << 16); }
DI unsigned pk_bf16(float lo, float hi) {
    typedef __bf16 bf2 __attribute__((ext_vector_type(2)));
    bf2 v; v.x = (__bf16)lo; v.y = (__bf16)hi;
    return __builtin_bit_cast(unsigned, v);
}
DI s16x4 tr_read(LAS unsigned char* p) { return __builtin_amdgcn_ds_read_tr16_b64_v4i16((LAS s16x4*)p); }
template <int OFF> DI s16x4 tr_read_a(unsigned addr) { s16x4 r; asm volatile("ds_read_b64_tr_b16 %0, %1 offset:%2" : "=v"(r) : "v"(addr), "i"(OFF)); return r; }
DI void glds16(const void* g, LAS unsigned char* l) {
    __builtin_amdgcn_global_load_lds((const __attribute__((address_space(1))) unsigned*)g, (LAS unsigned*)l, 16, 0, 0);
}
#define WAIT_V(n) asm volatile("s_waitcnt vmcnt(" #n ")" ::: "memory")
#define WAIT_L(n) asm volatile("s_waitcnt lgkmcnt(" #n ")" ::: "memory")
#define BAR __builtin_amdgcn_s_barrier()
#define SCHED __builtin_amdgcn_sched_barrier(0)
#define STAMP_DECL(kid)
#define STAMP_AT()
#define STAMP_RT()
#define STAMP_RT2()

__global__ __launch_bounds__(256) void ln_kernel(const bf16_t* __restrict__ in, bf16_t* __restrict__ out, const unsigned* __restrict__ pf, unsigned pf_lines,
                                                 const unsigned* __restrict__ pfb, unsigned pfb_lines) {
    const int row = blockIdx.x * 4 + (threadIdx.x >> 6), lane = threadIdx.x & 63;
    const u32x4* p = (const u32x4*)(in + (size_t)row * D_);
    u32x4 a = p[lane], b = p[lane + 64];
    { const unsigned li = blockIdx.x * 256u + threadIdx.x; if (li < pf_lines) { unsigned t = pf[(size_t)li * 32]; asm volatile("" :: "v"(t)); }
      else if (li - pf_lines < pfb_lines) { unsigned t = pfb[(size_t)(li - pf_lines) * 32]; asm volatile("" :: "v"(t)); } }
    float v[16];
#pragma unroll
    for (int i = 0; i < 4; ++i) { v[2 * i] = __uint_as_float(a[i] << 16); v[2 * i + 1] = __uint_as_float(a[i] & 0xffff0000u);
                                  v[8 + 2 * i] = __uint_as_float(b[i] << 16); v[8 + 2 * i + 1] = __uint_as_float(b[i] & 0xffff0000u); }
    float s = 0.f;
#pragma unroll
    for (int i = 0; i < 16; ++i) s += v[i];
#pragma unroll
    for (int o = 32; o >= 1; o >>= 1) s += __shfl_xor(s, o);
    const float mean = s * (1.0f / D_);
    float q = 0.f;
#pragma unroll
    for (int i = 0; i < 16; ++i) { v[i] -= mean; q += v[i] * v[i]; }
#pragma unroll
    for (int o = 32; o >= 1; o >>= 1) q += __shfl_xor(q, o);
    const float rstd = 1.0f / sqrtf(q * (1.0f / D_) + 1e-5f);
    u32x4 oa, ob;
#pragma unroll
    for (int i = 0; i < 4; ++i) { oa[i] = pk_bf16(v[2 * i] * rstd, v[2 * i + 1] * rstd); ob[i] = pk_bf16(v[8 + 2 * i] * rstd, v[8 + 2 * i + 1] * rstd); }
    u32x4* po = (u32x4*)(out + (size_t)row * D_);
    po[lane] = oa; po[lane + 64] = ob;
}

DI int bx_of_k(int k) { return ((k >> 1) & 1) | (((k >> 3) & 1) << 1); }

enum { EPI_QKV = 0, EPI_RES = 1 };

template <int N> DI void wait_v() { asm volatile("s_waitcnt vmcnt(%0)" :: "i"(N) : "memory"); }
template <int OFF> DI bf16x8 ld128_a(unsigned addr) { bf16x8 r; asm volatile("ds_read_b128 %0, %1 offset:%2" : "=v"(r) : "v"(addr), "i"(OFF)); return r; }
template <int BM, int BN, int WM, int WN, int NBUF, int EPI, int RM, int RN, int K>
__global__ __launch_bounds__(512, 2) void gemm_pp(const bf16_t* __restrict__ A, int lda, const bf16_t* __restrict__ W, int ldb,
                                                   const bf16_t* __restrict__ res, bf16_t* __restrict__ out, int tiles_m) {
    extern __shared__ __attribute__((aligned(16))) unsigned char smem_raw[];
    LAS unsigned char* lds = (LAS unsigned char*)smem_raw;
    constexpr int MT = BM / WM / 16, NT = BN / WN / 16;
    constexpr int NPA = BM / 64, NPB = BN / 64, NP = NPA + NPB;
    constexpr int ABYTES = BM * 128, TILE_BYTES = (BM + BN) * 128;
    constexpr int nt = K / 64;
    static_assert(WM * WN == 8, "8 waves");
    static_assert(MT == 2 || MT == 4, "MT");
    STAMP_DECL(EPI == EPI_QKV ? 0 : (K == 1024 ? 1 : 2)); STAMP_RT(); STAMP_AT();
    const int tid = threadIdx.x, wid = __builtin_amdgcn_readfirstlane(tid >> 6), lane = tid & 63;
    const int wm = wid / WN, wn = wid % WN, fr = lane & 15, fq = lane >> 4;
    int tm, tn;
    { const int bid = blockIdx.x, xcd = bid & 7, idx = bid >> 3; const int regm = tiles_m / RM;
      const int rr = xcd % regm, rc = xcd / regm; tm = rr * RM + (idx % RM); tn = rc * RN + (idx / RM); }
    const int m0 = tm * BM, n0 = tn * BN;
    const bf16_t* asrc[NPA]; const bf16_t* bsrc[NPB];
#pragma unroll
    for (int i = 0; i < NPA; ++i) { const int row = (wid + 8 * i) * 8 + (lane >> 3), ch = (lane & 7) ^ ((row >> 1) & 7);
        asrc[i] = A + (size_t)(m0 + row) * lda + ch * 8; }
#pragma unroll
    for (int j = 0; j < NPB; ++j) { const int k = wid * 8 + (lane >> 3), s16 = lane & 7, n16 = (s16 >> 1) ^ bx_of_k(k);
        bsrc[j] = W + (size_t)k * ldb + n0 + j * 64 + n16 * 16 + (s16 & 1) * 8; }
    const size_t bstep = (size_t)64 * ldb;
    auto stage = [&](int t, int buf) {
        LAS unsigned char* base = lds + buf * TILE_BYTES + wid * 1024;
#pragma unroll
        for (int i = 0; i < NPA; ++i) glds16(asrc[i] + (size_t)t * 64, base + i * 8192);
#pragma unroll
        for (int j = 0; j < NPB; ++j) glds16(bsrc[j] + (size_t)t * bstep, base + ABYTES + j * 8192);
    };
    const int sw = (fr >> 1) & 7;
    const unsigned aoff0 = (unsigned)(size_t)lds + (wm * MT * 16 + fr) * 128 + ((fq ^ sw) * 16), aoff1 = aoff0 ^ 64;
    unsigned boff[NT];
    { const int q = (lane & 15) >> 2, p = lane & 3, xl = ((q >> 1) & 1) | ((fq & 1) << 1);
#pragma unroll
      for (int n = 0; n < NT; ++n) { const int ntg = wn * NT + n;
          boff[n] = (unsigned)(size_t)lds + ABYTES + (ntg >> 2) * 8192 + (8 * fq + q) * 128 + (((ntg & 3) ^ xl) * 32) + p * 8; } }
    f32x4 acc[MT][NT];
#pragma unroll
    for (int m = 0; m < MT; ++m)
#pragma unroll
        for (int n = 0; n < NT; ++n) acc[m][n] = (f32x4){0.f, 0.f, 0.f, 0.f};
    bf16x8 afA[MT][2], afB[MT][2]; s16x4 bA[NT][4], bB[NT][4];
#define P_READ(AF, BR, buf) do { const unsigned bo_ = (unsigned)((buf) * TILE_BYTES); \
        _Pragma("unroll") for (int n = 0; n < NT; ++n) { const unsigned ba = boff[n] + bo_; \
            BR[n][0] = tr_read_a<0>(ba); BR[n][1] = tr_read_a<512>(ba); BR[n][2] = tr_read_a<4096>(ba); BR[n][3] = tr_read_a<4608>(ba); } \
        { const unsigned a0_ = aoff0 + bo_, a1_ = aoff1 + bo_; \
          AF[0][0] = ld128_a<0>(a0_); AF[0][1] = ld128_a<0>(a1_); AF[1][0] = ld128_a<2048>(a0_); AF[1][1] = ld128_a<2048>(a1_); \
          if constexpr (MT == 4) { AF[MT - 2][0] = ld128_a<4096>(a0_); AF[MT - 2][1] = ld128_a<4096>(a1_); AF[MT - 1][0] = ld128_a<6144>(a0_); AF[MT - 1][1] = ld128_a<6144>(a1_); } } } while (0)
#define P_WAITL(AF, BR) do { _Pragma("unroll") for (int n = 0; n < NT; ++n) \
        asm volatile("s_waitcnt lgkmcnt(0)" : "+v"(BR[n][0]), "+v"(BR[n][1]), "+v"(BR[n][2]), "+v"(BR[n][3]) :: "memory"); \
        _Pragma("unroll") for (int m = 0; m < MT; ++m) asm volatile("" : "+v"(AF[m][0]), "+v"(AF[m][1])); } while (0)
#define P_MMA(AF, BR) do { _Pragma("unroll") for (int kk = 0; kk < 2; ++kk) _Pragma("unroll") for (int m = 0; m < MT; ++m) _Pragma("unroll") for (int n = 0; n < NT; ++n) \
        acc[m][n] = __builtin_amdgcn_mfma_f32_16x16x32_bf16(__builtin_shufflevector(BR[n][2 * kk], BR[n][2 * kk + 1], 0, 1, 2, 3, 4, 5, 6, 7), AF[m][kk], acc[m][n], 0, 0, 0); } while (0)
#define P_STEP(AFC, BRC, AFN, BRN, FULL) do { \
        P_WAITL(AFC, BRC); \
        if (FULL || t + NBUF < nt) wait_v<(NBUF - 2) * NP>(); else wait_v<0>(); \
        BAR; \
        const int nb_ = (buf + 1 == NBUF) ? 0 : buf + 1; \
        if (FULL || t + 1 < nt) P_READ(AFN, BRN, nb_); \
        if (FULL || t + NBUF < nt) stage(t + NBUF, buf); \
        P_MMA(AFC, BRC); \
        buf = nb_; ++t; } while (0)
#pragma unroll
    for (int t = 0; t < NBUF; ++t) stage(t, t);
    wait_v<(NBUF - 1) * NP>();
    BAR;
    STAMP_AT();
    P_READ(afA, bA, 0);
    int buf = 0, t = 0;
    constexpr int nmain = ((nt - NBUF) / 2) * 2;
    for (; t < nmain;) {
        P_STEP(afA, bA, afB, bB, true);
        P_STEP(afB, bB, afA, bA, true);
    }
#pragma unroll
    for (int tt = nmain; tt < nt; tt += 2) {
        t = tt;
        P_STEP(afA, bA, afB, bB, false);
        P_STEP(afB, bB, afA, bA, false);
    }
    STAMP_AT();
#undef P_READ
#undef P_WAITL
#undef P_MMA
#undef P_STEP
#pragma unroll
    for (int m = 0; m < MT; ++m) {
        const int row = m0 + (wm * MT + m) * 16 + fr;
#pragma unroll
        for (int n = 0; n < NT; ++n) {
            const int col = n0 + (wn * NT + n) * 16 + 4 * fq;
            f32x4 v = acc[m][n];
            if constexpr (EPI == EPI_QKV) {
                u32x2 w; w.x = pk_bf16(v[0], v[1]); w.y = pk_bf16(v[2], v[3]);
                *(u32x2*)(out + ((size_t)(col >> 5) * S_ + row) * 32 + (col & 31)) = w;
            } else {
                const u32x2 r = *(const u32x2*)(res + (size_t)row * D_ + col);
                v[0] += __uint_as_float(r.x << 16); v[1] += __uint_as_float(r.x & 0xffff0000u);
                v[2] += __uint_as_float(r.y << 16); v[3] += __uint_as_float(r.y & 0xffff0000u);
                u32x2 w; w.x = pk_bf16(v[0], v[1]); w.y = pk_bf16(v[2], v[3]);
                *(u32x2*)(out + (size_t)row * D_ + col) = w;
            }
        }
    }
    WAIT_V(0); STAMP_AT(); STAMP_RT2();
}

__global__ __launch_bounds__(512, 2) void gemm_gateup(const bf16_t* __restrict__ A, const bf16_t* __restrict__ W1, const bf16_t* __restrict__ W3,
                                                       bf16_t* __restrict__ act) {
    extern __shared__ __attribute__((aligned(16))) unsigned char smem_raw[];
    LAS unsigned char* lds = (LAS unsigned char*)smem_raw;
    constexpr int K = D_, HTB = 16384;
    STAMP_DECL(3); STAMP_RT(); STAMP_AT();
    const int tid = threadIdx.x, wid = __builtin_amdgcn_readfirstlane(tid >> 6), lane = tid & 63;
    const int wr = wid >> 2, wc = wid & 3, fr = lane & 15, fq = lane >> 4;
    int tm, tn;
    { const int bid = blockIdx.x, xcd = bid & 7, idx = bid >> 3; tm = (xcd & 1) * 4 + (idx & 3); tn = (xcd >> 1) * 7 + (idx >> 2); }
    const int m0 = tm * 256, f0 = tn * 128;
    constexpr int nt = K / 64;
    unsigned aoffs[2], boffs[2];
#pragma unroll
    for (int i = 0; i < 2; ++i) { const int row = (wid + 8 * i) * 8 + (lane >> 3), ch = (lane & 7) ^ ((row >> 1) & 7);
        aoffs[i] = (unsigned)(row * K + ch * 8) * 2u;
        const int k = wid * 8 + (lane >> 3), s16 = lane & 7, n16 = (s16 >> 1) ^ bx_of_k(k);
        boffs[i] = (unsigned)(k * F_ + i * 64 + n16 * 16 + (s16 & 1) * 8) * 2u; }
    const char* gA0 = (const char*)(A + (size_t)m0 * K); const char* gA1 = gA0 + (size_t)128 * K * 2;
    const char* gB0 = (const char*)(W1 + f0); const char* gB1 = (const char*)(W3 + f0);
    constexpr size_t akstep = 128, bkstep = (size_t)64 * F_ * 2;
    const unsigned ldsw = (unsigned)wid * 1024u;
#define G_SA(b, h) (((b) * 2 + (h)) * HTB)
#define G_SB(b, h) ((4 + (b) * 2 + (h)) * HTB)
#define G_STAGE(bufoff, gbase, voff) do { _Pragma("unroll") for (int _i = 0; _i < 2; ++_i) glds16((gbase) + (voff)[_i], lds + (bufoff) + ldsw + _i * 8192); } while (0)
    const int sw = (fr >> 1) & 7;
    const int aoff0 = (wr * 64 + fr) * 128 + ((fq ^ sw) * 16), aoff1 = aoff0 ^ 64;
    int boff[2];
    { const int q = (lane & 15) >> 2, p = lane & 3, xl = ((q >> 1) & 1) | ((fq & 1) << 1);
#pragma unroll
      for (int n = 0; n < 2; ++n) { const int ntg = wc * 2 + n; boff[n] = (ntg >> 2) * 8192 + (8 * fq + q) * 128 + (((ntg & 3) ^ xl) * 32) + p * 8; } }
#define G_LDA(dst, b, h) do { _Pragma("unroll") for (int m = 0; m < 4; ++m) { dst[m][0] = *(const LAS bf16x8*)(lds + G_SA(b, h) + aoff0 + m * 2048); \
        dst[m][1] = *(const LAS bf16x8*)(lds + G_SA(b, h) + aoff1 + m * 2048); } } while (0)
#define G_LDB(dst, b, h) do { _Pragma("unroll") for (int n = 0; n < 2; ++n) { \
        dst[n][0] = tr_read_a<G_SA(b, h)>(lbase + boff[n]); dst[n][1] = tr_read_a<G_SA(b, h) + 512>(lbase + boff[n]); \
        dst[n][2] = tr_read_a<G_SA(b, h) + 4096>(lbase + boff[n]); dst[n][3] = tr_read_a<G_SA(b, h) + 4608>(lbase + boff[n]); } } while (0)
#define G_WAITB(R) do { asm volatile("s_waitcnt lgkmcnt(0)" : "+v"(R[0][0]), "+v"(R[0][1]), "+v"(R[0][2]), "+v"(R[0][3]), "+v"(R[1][0]), "+v"(R[1][1]), "+v"(R[1][2]), "+v"(R[1][3]) :: "memory"); } while (0)
#define G_FR(R, n, k) __builtin_shufflevector(R[n][2 * (k)], R[n][2 * (k) + 1], 0, 1, 2, 3, 4, 5, 6, 7)
#define G_MMA(ai, bj, At, Bt) do { __builtin_amdgcn_s_setprio(1); _Pragma("unroll") for (int m = 0; m < 4; ++m) _Pragma("unroll") for (int n = 0; n < 2; ++n) \
        _Pragma("unroll") for (int k = 0; k < 2; ++k) acc[ai][bj][m][n] = __builtin_amdgcn_mfma_f32_16x16x32_bf16(G_FR(Bt, n, k), At[m][k], acc[ai][bj][m][n], 0, 0, 0); \
        __builtin_amdgcn_s_setprio(0); } while (0)
    f32x4 acc[2][2][4][2];
#pragma unroll
    for (int a = 0; a < 2; ++a)
#pragma unroll
        for (int b = 0; b < 2; ++b)
#pragma unroll
            for (int m = 0; m < 4; ++m)
#pragma unroll
                for (int n = 0; n < 2; ++n) acc[a][b][m][n] = (f32x4){0.f, 0.f, 0.f, 0.f};
    bf16x8 At[4][2]; s16x4 B0[2][4], B1[2][4];
    const unsigned lbase = (unsigned)(size_t)lds + 4 * HTB;
    G_STAGE(G_SB(0, 0), gB0, boffs); G_STAGE(G_SA(0, 0), gA0, aoffs); G_STAGE(G_SB(0, 1), gB1, boffs); G_STAGE(G_SA(0, 1), gA1, aoffs);
    if (wr == 1) BAR;
    WAIT_V(4); BAR;
    G_STAGE(G_SB(1, 0), gB0 + bkstep, boffs); G_STAGE(G_SA(1, 0), gA0 + akstep, aoffs); G_STAGE(G_SB(1, 1), gB1 + bkstep, boffs);
    WAIT_V(6); BAR;
    STAMP_AT();
    for (int t = 0; t < nt; t += 2) {
        const int t1 = t + 1, t2 = (t + 2 < nt) ? t + 2 : nt - 1, t3 = (t + 3 < nt) ? t + 3 : nt - 1;
        G_LDB(B0, 0, 0); SCHED; G_LDA(At, 0, 0); G_STAGE(G_SA(1, 1), gA1 + t1 * akstep, aoffs);
        WAIT_L(8); BAR; G_WAITB(B0); G_MMA(0, 0, At, B0); BAR; SCHED;
        G_LDB(B1, 0, 1); G_STAGE(G_SB(0, 0), gB0 + t2 * bkstep, boffs);
        BAR; G_WAITB(B1); G_MMA(0, 1, At, B1); BAR;
        G_LDA(At, 0, 1); G_STAGE(G_SA(0, 0), gA0 + t2 * akstep, aoffs);
        BAR; WAIT_L(0); G_MMA(1, 0, At, B0); BAR; SCHED;
        G_STAGE(G_SB(0, 1), gB1 + t2 * bkstep, boffs);
        WAIT_V(6); BAR; G_MMA(1, 1, At, B1); BAR;
        G_LDB(B0, 1, 0); SCHED; G_LDA(At, 1, 0); G_STAGE(G_SA(0, 1), gA1 + t2 * akstep, aoffs);
        WAIT_L(8); BAR; G_WAITB(B0); G_MMA(0, 0, At, B0); BAR; SCHED;
        G_LDB(B1, 1, 1); G_STAGE(G_SB(1, 0), gB0 + t3 * bkstep, boffs);
        BAR; G_WAITB(B1); G_MMA(0, 1, At, B1); BAR;
        G_LDA(At, 1, 1); G_STAGE(G_SA(1, 0), gA0 + t3 * akstep, aoffs);
        BAR; WAIT_L(0); G_MMA(1, 0, At, B0); BAR; SCHED;
        G_STAGE(G_SB(1, 1), gB1 + t3 * bkstep, boffs);
        WAIT_V(6); BAR; G_MMA(1, 1, At, B1); BAR;
    }
    STAMP_AT();
    WAIT_V(0);
    if (wr == 0) BAR;
#pragma unroll
    for (int ai = 0; ai < 2; ++ai)
#pragma unroll
        for (int m = 0; m < 4; ++m) {
            const int row = m0 + ai * 128 + wr * 64 + m * 16 + fr;
#pragma unroll
            for (int n = 0; n < 2; ++n) {
                const f32x4 g = acc[ai][0][m][n], u = acc[ai][1][m][n];
                float o[4];
#pragma unroll
                for (int j = 0; j < 4; ++j) o[j] = g[j] / (1.0f + __expf(-g[j])) * u[j];
                u32x2 w; w.x = pk_bf16(o[0], o[1]); w.y = pk_bf16(o[2], o[3]);
                *(u32x2*)(act + (size_t)row * F_ + f0 + wc * 32 + n * 16 + 4 * fq) = w;
            }
        }
    WAIT_V(0); STAMP_AT(); STAMP_RT2();
#undef G_SA
#undef G_SB
#undef G_STAGE
#undef G_LDA
#undef G_LDB
#undef G_MMA
#undef G_WAITB
#undef G_FR
}

constexpr int AT_ROWS = 336;
__global__ __launch_bounds__(256) void attn_kernel(const bf16_t* __restrict__ qkv, bf16_t* __restrict__ o, const unsigned* __restrict__ pf0,
                                                   const unsigned* __restrict__ pf1, const unsigned* __restrict__ pf2) {
    __shared__ __attribute__((aligned(16))) unsigned char sm[2 * AT_ROWS * 64];
    LAS unsigned char* lk = (LAS unsigned char*)sm; LAS unsigned char* lv = lk + AT_ROWS * 64;
    const int tid = threadIdx.x, wid = __builtin_amdgcn_readfirstlane(tid >> 6), lane = tid & 63, fr = lane & 15, fq = lane >> 4;
    const int head = blockIdx.x & 31, qb = blockIdx.x >> 5, q0 = qb * 64;
    const bf16_t* Q = qkv + (size_t)head * S_ * 32;
    const bf16_t* Kg = qkv + (size_t)(32 + head) * S_ * 32;
    const bf16_t* Vg = qkv + (size_t)(64 + head) * S_ * 32;
    for (int pc = wid; pc < AT_ROWS / 16; pc += 4) {
        const int row = pc * 16 + (lane >> 2), slot = lane & 3;
        int key = q0 - 256 + row; key = key < 0 ? 0 : (key > S_ - 1 ? S_ - 1 : key);
        const int chk = slot ^ ((-(row >> 2)) & 3), chv = slot ^ (((row >> 2) & 1) << 1);
        glds16(Kg + (size_t)key * 32 + chk * 8, lk + pc * 1024);
        glds16(Vg + (size_t)key * 32 + chv * 8, lv + pc * 1024);
    }
    const int qrow = q0 + wid * 16 + fr;
    const bf16x8 qf = *(const bf16x8*)(Q + (size_t)qrow * 32 + fq * 8);
    WAIT_V(0);
    __syncthreads();
    unsigned pfv = 0;
    { const unsigned li = blockIdx.x * 256u + threadIdx.x; constexpr unsigned NL = (unsigned)((size_t)D_ * F_ * 2 / 128);
      if (li < 3 * NL) { const unsigned* pf = li < NL ? pf0 : (li < 2 * NL ? pf1 : pf2); const unsigned l2 = li < NL ? li : (li < 2 * NL ? li - NL : li - 2 * NL);
          pfv = pf[(size_t)l2 * 32]; } }
    const int kbase = q0 + wid * 16 - 256;
    const int istart = kbase < 0 ? (-kbase) >> 4 : 0;
    f32x4 s[17];
    const int koff = (wid * 16 + fr) * 64 + ((fq ^ ((-(fr >> 2)) & 3)) * 16);
#pragma unroll
    for (int i = 0; i < 17; ++i) {
        const bf16x8 kf = *(const LAS bf16x8*)(lk + koff + i * 1024);
        s[i] = __builtin_amdgcn_mfma_f32_16x16x32_bf16(kf, qf, (f32x4){0.f, 0.f, 0.f, 0.f}, 0, 0, 0);
    }
    const float NEG = -3.0e38f;
#pragma unroll
    for (int r = 0; r < 4; ++r) { if (!(4 * fq + r > fr)) s[0][r] = NEG; if (!(4 * fq + r <= fr)) s[16][r] = NEG; }
#pragma unroll
    for (int i = 0; i < 16; ++i) if (i < istart) s[i] = (f32x4){NEG, NEG, NEG, NEG};
    float mx = NEG;
#pragma unroll
    for (int i = 0; i < 17; ++i) mx = fmaxf(mx, fmaxf(fmaxf(s[i][0], s[i][1]), fmaxf(s[i][2], s[i][3])));
    mx = fmaxf(mx, __shfl_xor(mx, 16)); mx = fmaxf(mx, __shfl_xor(mx, 32));
    const float c = 0.17677669529663687f * 1.4426950408889634f;
    const float mc = mx * c;
    float sum = 0.f;
#pragma unroll
    for (int i = 0; i < 17; ++i)
#pragma unroll
        for (int r = 0; r < 4; ++r) { const float p = __builtin_amdgcn_exp2f(s[i][r] * c - mc); s[i][r] = p; sum += p; }
    sum += __shfl_xor(sum, 16); sum += __shfl_xor(sum, 32);
    f32x4 oacc[2] = {(f32x4){0.f, 0.f, 0.f, 0.f}, (f32x4){0.f, 0.f, 0.f, 0.f}};
    const int q4 = (lane & 15) >> 2, p4 = lane & 3;
    const int voff = (wid * 16 + 4 * fq + q4) * 64 + p4 * 8;
    const int vx = (fq & 1) << 5;
#pragma unroll
    for (int cc = 0; cc < 9; ++cc) {
        u32x4 pw; pw.x = pk_bf16(s[2 * cc][0], s[2 * cc][1]); pw.y = pk_bf16(s[2 * cc][2], s[2 * cc][3]);
        if (cc < 8) { pw.z = pk_bf16(s[2 * cc + 1][0], s[2 * cc + 1][1]); pw.w = pk_bf16(s[2 * cc + 1][2], s[2 * cc + 1][3]); } else { pw.z = 0u; pw.w = 0u; }
        const bf16x8 pf = __builtin_bit_cast(bf16x8, pw);
#pragma unroll
        for (int dt = 0; dt < 2; ++dt) {
            const s16x4 lo = tr_read(lv + voff + cc * 2048 + ((dt * 32) ^ vx)), hi = tr_read(lv + voff + cc * 2048 + 1024 + ((dt * 32) ^ vx));
            const bf16x8 vf = __builtin_shufflevector(lo, hi, 0, 1, 2, 3, 4, 5, 6, 7);
            oacc[dt] = __builtin_amdgcn_mfma_f32_16x16x32_bf16(vf, pf, oacc[dt], 0, 0, 0);
        }
    }
    const float inv = 1.0f / sum;
#pragma unroll
    for (int dt = 0; dt < 2; ++dt) {
        u32x2 w; w.x = pk_bf16(oacc[dt][0] * inv, oacc[dt][1] * inv); w.y = pk_bf16(oacc[dt][2] * inv, oacc[dt][3] * inv);
        *(u32x2*)(o + (size_t)qrow * D_ + head * 32 + dt * 16 + 4 * fq) = w;
    }
    asm volatile("" :: "v"(pfv));
}

extern "C" void kernel_launch(void* const* d_in, const int* in_sizes, int n_in, void* d_out, int out_size, void* d_ws, size_t ws_size,
                              hipStream_t stream) {
    const bf16_t* x = (const bf16_t*)d_in[0];
    const bf16_t* wqkv = (const bf16_t*)d_in[1];
    const bf16_t* wo = (const bf16_t*)d_in[2];
    const bf16_t* w1 = (const bf16_t*)d_in[4];
    const bf16_t* w3 = (const bf16_t*)d_in[5];
    const bf16_t* w2 = (const bf16_t*)d_in[6];
    bf16_t* out = (bf16_t*)d_out;
    char* ws = (char*)d_ws;
    bf16_t* xn = (bf16_t*)(ws);
    bf16_t* qkv = (bf16_t*)(ws + (size_t)4 * 1024 * 1024);
    bf16_t* o = (bf16_t*)(ws + (size_t)20 * 1024 * 1024);
    bf16_t* h = (bf16_t*)(ws + (size_t)24 * 1024 * 1024);
    bf16_t* hn = (bf16_t*)(ws + (size_t)28 * 1024 * 1024);
    bf16_t* act = (bf16_t*)(ws + (size_t)32 * 1024 * 1024);

    ln_kernel<<<S_ / 4, 256, 0, stream>>>(x, xn, (const unsigned*)wqkv, (unsigned)((size_t)D_ * 3 * D_ * 2 / 128),
                                          (const unsigned*)wo, (unsigned)((size_t)D_ * D_ * 2 / 128));
    {
        auto kern = gemm_pp<128, 192, 2, 4, 3, EPI_QKV, 8, 4, 1024>;
        const int shm = 3 * (128 + 192) * 128;
        (void)hipFuncSetAttribute((const void*)kern, hipFuncAttributeMaxDynamicSharedMemorySize, shm);
        kern<<<256, 512, shm, stream>>>(xn, D_, wqkv, 3 * D_, nullptr, qkv, 16);
    }
    attn_kernel<<<32 * 32, 256, 0, stream>>>(qkv, o, (const unsigned*)w1, (const unsigned*)w3, (const unsigned*)w2);
    {
        auto kern = gemm_pp<128, 64, 4, 2, 6, EPI_RES, 4, 8, 1024>;
        const int shm = 6 * (128 + 64) * 128;
        (void)hipFuncSetAttribute((const void*)kern, hipFuncAttributeMaxDynamicSharedMemorySize, shm);
        kern<<<256, 512, shm, stream>>>(o, D_, wo, D_, x, h, 16);
    }
    ln_kernel<<<S_ / 4, 256, 0, stream>>>(h, hn, (const unsigned*)w2, 0u, (const unsigned*)w2, 0u);
    {
        const int shm = 8 * 16384;
        (void)hipFuncSetAttribute((const void*)gemm_gateup, hipFuncAttributeMaxDynamicSharedMemorySize, shm);
        gemm_gateup<<<224, 512, shm, stream>>>(hn, w1, w3, act);
    }
    {
        auto kern = gemm_pp<128, 64, 4, 2, 6, EPI_RES, 4, 8, 3584>;
        const int shm = 6 * (128 + 64) * 128;
        (void)hipFuncSetAttribute((const void*)kern, hipFuncAttributeMaxDynamicSharedMemorySize, shm);
        kern<<<256, 512, shm, stream>>>(act, F_, w2, D_, h, out, 16);
    }
}
```

```cpp
#include <hip/hip_runtime.h>
#include <hip/hip_bf16.h>
#include <stdint.h>

typedef unsigned short bf16_t;
typedef short bf16x8 __attribute__((ext_vector_type(8)));
typedef short s16x4 __attribute__((ext_vector_type(4)));
typedef float f32x4 __attribute__((ext_vector_type(4)));
typedef unsigned u32x4 __attribute__((ext_vector_type(4)));
typedef unsigned u32x2 __attribute__((ext_vector_type(2)));
#define LAS __attribute__((address_space(3)))
#define DI __device__ __forceinline__

constexpr int S_ = 2048, D_ = 1024, F_ = 3584, H_ = 32, DH_ = 32, WIN_ = 256;

DI float bf2f(unsigned short b) { return __uint_as_float(((unsigned)b) << 16); }
DI unsigned pk_bf16(float lo, float hi) {
    typedef __bf16 bf2 __attribute__((ext_vector_type(2)));
    bf2 v; v.x = (__bf16)lo; v.y = (__bf16)hi;
    return __builtin_bit_cast(unsigned, v);
}
DI s16x4 tr_read(LAS unsigned char* p) { return __builtin_amdgcn_ds_read_tr16_b64_v4i16((LAS s16x4*)p); }
template <int OFF> DI s16x4 tr_read_a(unsigned addr) { s16x4 r; asm volatile("ds_read_b64_tr_b16 %0, %1 offset:%2" : "=v"(r) : "v"(addr), "i"(OFF)); return r; }
DI void glds16(const void* g, LAS unsigned char* l) {
    __builtin_amdgcn_global_load_lds((const __attribute__((address_space(1))) unsigned*)g, (LAS unsigned*)l, 16, 0, 0);
}
#define WAIT_V(n) asm volatile("s_waitcnt vmcnt(" #n ")" ::: "memory")
#define WAIT_L(n) asm volatile("s_waitcnt lgkmcnt(" #n ")" ::: "memory")
#define BAR __builtin_amdgcn_s_barrier()
#define SCHED __builtin_amdgcn_sched_barrier(0)
#define STAMP_DECL(kid)
#define STAMP_AT()
#define STAMP_RT()
#define STAMP_RT2()

__global__ __launch_bounds__(256) void ln_kernel(const bf16_t* __restrict__ in, bf16_t* __restrict__ out, const unsigned* __restrict__ pf, unsigned pf_lines,
                                                 const unsigned* __restrict__ pfb, unsigned pfb_lines) {
    const int row = blockIdx.x * 4 + (threadIdx.x >> 6), lane = threadIdx.x & 63;
    const u32x4* p = (const u32x4*)(in + (size_t)row * D_);
    u32x4 a = p[lane], b = p[lane + 64];
    { const unsigned li = blockIdx.x * 256u + threadIdx.x; if (li < pf_lines) { unsigned t = pf[(size_t)li * 32]; asm volatile("" :: "v"(t)); }
      else if (li - pf_lines < pfb_lines) { unsigned t = pfb[(size_t)(li - pf_lines) * 32]; asm volatile("" :: "v"(t)); } }
    float v[16];
#pragma unroll
    for (int i = 0; i < 4; ++i) { v[2 * i] = __uint_as_float(a[i] << 16); v[2 * i + 1] = __uint_as_float(a[i] & 0xffff0000u);
                                  v[8 + 2 * i] = __uint_as_float(b[i] << 16); v[8 + 2 * i + 1] = __uint_as_float(b[i] & 0xffff0000u); }
    float s = 0.f;
#pragma unroll
    for (int i = 0; i < 16; ++i) s += v[i];
#pragma unroll
    for (int o = 32; o >= 1; o >>= 1) s += __shfl_xor(s, o);
    const float mean = s * (1.0f / D_);
    float q = 0.f;
#pragma unroll
    for (int i = 0; i < 16; ++i) { v[i] -= mean; q += v[i] * v[i]; }
#pragma unroll
    for (int o = 32; o >= 1; o >>= 1) q += __shfl_xor(q, o);
    const float rstd = 1.0f / sqrtf(q * (1.0f / D_) + 1e-5f);
    u32x4 oa, ob;
#pragma unroll
    for (int i = 0; i < 4; ++i) { oa[i] = pk_bf16(v[2 * i] * rstd, v[2 * i + 1] * rstd); ob[i] = pk_bf16(v[8 + 2 * i] * rstd, v[8 + 2 * i + 1] * rstd); }
    u32x4* po = (u32x4*)(out + (size_t)row * D_);
    po[lane] = oa; po[lane + 64] = ob;
}

DI int bx_of_k(int k) { return ((k >> 1) & 1) | (((k >> 3) & 1) << 1); }

enum { EPI_QKV = 0, EPI_RES = 1 };

template <int N> DI void wait_v() { asm volatile("s_waitcnt vmcnt(%0)" :: "i"(N) : "memory"); }
template <int OFF> DI bf16x8 ld128_a(unsigned addr) { bf16x8 r; asm volatile("ds_read_b128 %0, %1 offset:%2" : "=v"(r) : "v"(addr), "i"(OFF)); return r; }
template <int BM, int BN, int WM, int WN, int NBUF, int EPI, int RM, int RN, int K>
__global__ __launch_bounds__(512, 2) void gemm_pp(const bf16_t* __restrict__ A, int lda, const bf16_t* __restrict__ W, int ldb,
                                                   const bf16_t* __restrict__ res, bf16_t* __restrict__ out, int tiles_m) {
    extern __shared__ __attribute__((aligned(16))) unsigned char smem_raw[];
    LAS unsigned char* lds = (LAS unsigned char*)smem_raw;
    constexpr int MT = BM / WM / 16, NT = BN / WN / 16;
    constexpr int NPA = BM / 64, NPB = BN / 64, NP = NPA + NPB;
    constexpr int ABYTES = BM * 128, TILE_BYTES = (BM + BN) * 128;
    constexpr int nt = K / 64;
    static_assert(WM * WN == 8, "8 waves");
    static_assert(MT == 2 || MT == 4, "MT");
    STAMP_DECL(EPI == EPI_QKV ? 0 : (K == 1024 ? 1 : 2)); STAMP_RT(); STAMP_AT();
    const int tid = threadIdx.x, wid = __builtin_amdgcn_readfirstlane(tid >> 6), lane = tid & 63;
    const int wm = wid / WN, wn = wid % WN, fr = lane & 15, fq = lane >> 4;
    int tm, tn;
    { const int bid = blockIdx.x, xcd = bid & 7, idx = bid >> 3; const int regm = tiles_m / RM;
      const int rr = xcd % regm, rc = xcd / regm; tm = rr * RM + (idx % RM); tn = rc * RN + (idx / RM); }
    const int m0 = tm * BM, n0 = tn * BN;
    const bf16_t* asrc[NPA]; const bf16_t* bsrc[NPB];
#pragma unroll
    for (int i = 0; i < NPA; ++i) { const int row = (wid + 8 * i) * 8 + (lane >> 3), ch = (lane & 7) ^ ((row >> 1) & 7);
        asrc[i] = A + (size_t)(m0 + row) * lda + ch * 8; }
#pragma unroll
    for (int j = 0; j < NPB; ++j) { const int k = wid * 8 + (lane >> 3), s16 = lane & 7, n16 = (s16 >> 1) ^ bx_of_k(k);
        bsrc[j] = W + (size_t)k * ldb + n0 + j * 64 + n16 * 16 + (s16 & 1) * 8; }
    const size_t bstep = (size_t)64 * ldb;
    auto stage = [&](int t, int buf) {
        LAS unsigned char* base = lds + buf * TILE_BYTES + wid * 1024;
#pragma unroll
        for (int i = 0; i < NPA; ++i) glds16(asrc[i] + (size_t)t * 64, base + i * 8192);
#pragma unroll
        for (int j = 0; j < NPB; ++j) glds16(bsrc[j] + (size_t)t * bstep, base + ABYTES + j * 8192);
    };
    const int sw = (fr >> 1) & 7;
    const unsigned aoff0 = (unsigned)(size_t)lds + (wm * MT * 16 + fr) * 128 + ((fq ^ sw) * 16), aoff1 = aoff0 ^ 64;
    unsigned boff[NT];
    { const int q = (lane & 15) >> 2, p = lane & 3, xl = ((q >> 1) & 1) | ((fq & 1) << 1);
#pragma unroll
      for (int n = 0; n < NT; ++n) { const int ntg = wn * NT + n;
          boff[n] = (unsigned)(size_t)lds + ABYTES + (ntg >> 2) * 8192 + (8 * fq + q) * 128 + (((ntg & 3) ^ xl) * 32) + p * 8; } }
    f32x4 acc[MT][NT];
#pragma unroll
    for (int m = 0; m < MT; ++m)
#pragma unroll
        for (int n = 0; n < NT; ++n) acc[m][n] = (f32x4){0.f, 0.f, 0.f, 0.f};
    bf16x8 afA[MT][2], afB[MT][2]; s16x4 bA[NT][4], bB[NT][4];
#define P_READ(AF, BR, buf) do { const unsigned bo_ = (unsigned)((buf) * TILE_BYTES); \
        _Pragma("unroll") for (int n = 0; n < NT; ++n) { const unsigned ba = boff[n] + bo_; \
            BR[n][0] = tr_read_a<0>(ba); BR[n][1] = tr_read_a<512>(ba); BR[n][2] = tr_read_a<4096>(ba); BR[n][3] = tr_read_a<4608>(ba); } \
        { const unsigned a0_ = aoff0 + bo_, a1_ = aoff1 + bo_; \
          AF[0][0] = ld128_a<0>(a0_); AF[0][1] = ld128_a<0>(a1_); AF[1][0] = ld128_a<2048>(a0_); AF[1][1] = ld128_a<2048>(a1_); \
          if constexpr (MT == 4) { AF[MT - 2][0] = ld128_a<4096>(a0_); AF[MT - 2][1] = ld128_a<4096>(a1_); AF[MT - 1][0] = ld128_a<6144>(a0_); AF[MT - 1][1] = ld128_a<6144>(a1_); } } } while (0)
#define P_WAITL(AF, BR) do { _Pragma("unroll") for (int n = 0; n < NT; ++n) \
        asm volatile("s_waitcnt lgkmcnt(0)" : "+v"(BR[n][0]), "+v"(BR[n][1]), "+v"(BR[n][2]), "+v"(BR[n][3]) :: "memory"); \
        _Pragma("unroll") for (int m = 0; m < MT; ++m) asm volatile("" : "+v"(AF[m][0]), "+v"(AF[m][1])); } while (0)
#define P_MMA(AF, BR) do { _Pragma("unroll") for (int kk = 0; kk < 2; ++kk) _Pragma("unroll") for (int m = 0; m < MT; ++m) _Pragma("unroll") for (int n = 0; n < NT; ++n) \
        acc[m][n] = __builtin_amdgcn_mfma_f32_16x16x32_bf16(__builtin_shufflevector(BR[n][2 * kk], BR[n][2 * kk + 1], 0, 1, 2, 3, 4, 5, 6, 7), AF[m][kk], acc[m][n], 0, 0, 0); } while (0)
#define P_STEP(AFC, BRC, AFN, BRN, FULL) do { \
        P_WAITL(AFC, BRC); \
        if (FULL || t + NBUF < nt) wait_v<(NBUF - 2) * NP>(); else wait_v<0>(); \
        BAR; \
        const int nb_ = (buf + 1 == NBUF) ? 0 : buf + 1; \
        if (FULL || t + 1 < nt) P_READ(AFN, BRN, nb_); \
        if (FULL || t + NBUF < nt) stage(t + NBUF, buf); \
        P_MMA(AFC, BRC); \
        buf = nb_; ++t; } while (0)
#pragma unroll
    for (int t = 0; t < NBUF; ++t) stage(t, t);
    wait_v<(NBUF - 1) * NP>();
    BAR;
    STAMP_AT();
    P_READ(afA, bA, 0);
    int buf = 0, t = 0;
    constexpr int nmain = ((nt - NBUF) / 2) * 2;
    for (; t < nmain;) {
        P_STEP(afA, bA, afB, bB, true);
        P_STEP(afB, bB, afA, bA, true);
    }
#pragma unroll
    for (int tt = nmain; tt < nt; tt += 2) {
        t = tt;
        P_STEP(afA, bA, afB, bB, false);
        P_STEP(afB, bB, afA, bA, false);
    }
    STAMP_AT();
#undef P_READ
#undef P_WAITL
#undef P_MMA
#undef P_STEP
#pragma unroll
    for (int m = 0; m < MT; ++m) {
        const int row = m0 + (wm * MT + m) * 16 + fr;
#pragma unroll
        for (int n = 0; n < NT; ++n) {
            const int col = n0 + (wn * NT + n) * 16 + 4 * fq;
            f32x4 v = acc[m][n];
            if constexpr (EPI == EPI_QKV) {
                u32x2 w; w.x = pk_bf16(v[0], v[1]); w.y = pk_bf16(v[2], v[3]);
                *(u32x2*)(out + ((size_t)(col >> 5) * S_ + row) * 32 + (col & 31)) = w;
            } else {
                const u32x2 r = *(const u32x2*)(res + (size_t)row * D_ + col);
                v[0] += __uint_as_float(r.x << 16); v[1] += __uint_as_float(r.x & 0xffff0000u);
                v[2] += __uint_as_float(r.y << 16); v[3] += __uint_as_float(r.y & 0xffff0000u);
                u32x2 w; w.x = pk_bf16(v[0], v[1]); w.y = pk_bf16(v[2], v[3]);
                *(u32x2*)(out + (size_t)row * D_ + col) = w;
            }
        }
    }
    WAIT_V(0); STAMP_AT(); STAMP_RT2();
}

__global__ __launch_bounds__(512, 2) void gemm_gateup(const bf16_t* __restrict__ A, const bf16_t* __restrict__ W1, const bf16_t* __restrict__ W3,
                                                       bf16_t* __restrict__ act) {
    extern __shared__ __attribute__((aligned(16))) unsigned char smem_raw[];
    LAS unsigned char* lds = (LAS unsigned char*)smem_raw;
    constexpr int K = D_, HTB = 16384;
    STAMP_DECL(3); STAMP_RT(); STAMP_AT();
    const int tid = threadIdx.x, wid = __builtin_amdgcn_readfirstlane(tid >> 6), lane = tid & 63;
    const int wr = wid >> 2, wc = wid & 3, fr = lane & 15, fq = lane >> 4;
    int tm, tn;
    { const int bid = blockIdx.x, xcd = bid & 7, idx = bid >> 3; tm = (xcd & 1) * 4 + (idx & 3); tn = (xcd >> 1) * 7 + (idx >> 2); }
    const int m0 = tm * 256, f0 = tn * 128;
    constexpr int nt = K / 64;
    unsigned aoffs[2], boffs[2];
#pragma unroll
    for (int i = 0; i < 2; ++i) { const int row = (wid + 8 * i) * 8 + (lane >> 3), ch = (lane & 7) ^ ((row >> 1) & 7);
        aoffs[i] = (unsigned)(row * K + ch * 8) * 2u;
        const int k = wid * 8 + (lane >> 3), s16 = lane & 7, n16 = (s16 >> 1) ^ bx_of_k(k);
        boffs[i] = (unsigned)(k * F_ + i * 64 + n16 * 16 + (s16 & 1) * 8) * 2u; }
    const char* gA0 = (const char*)(A + (size_t)m0 * K); const char* gA1 = gA0 + (size_t)128 * K * 2;
    const char* gB0 = (const char*)(W1 + f0); const char* gB1 = (const char*)(W3 + f0);
    constexpr size_t akstep = 128, bkstep = (size_t)64 * F_ * 2;
    const unsigned ldsw = (unsigned)wid * 1024u;
#define G_SA(b, h) (((b) * 2 + (h)) * HTB)
#define G_SB(b, h) ((4 + (b) * 2 + (h)) * HTB)
#define G_STAGE(bufoff, gbase, voff) do { _Pragma("unroll") for (int _i = 0; _i < 2; ++_i) glds16((gbase) + (voff)[_i], lds + (bufoff) + ldsw + _i * 8192); } while (0)
    const int sw = (fr >> 1) & 7;
    const int aoff0 = (wr * 64 + fr) * 128 + ((fq ^ sw) * 16), aoff1 = aoff0 ^ 64;
    int boff[2];
    { const int q = (lane & 15) >> 2, p = lane & 3, xl = ((q >> 1) & 1) | ((fq & 1) << 1);
#pragma unroll
      for (int n = 0; n < 2; ++n) { const int ntg = wc * 2 + n; boff[n] = (ntg >> 2) * 8192 + (8 * fq + q) * 128 + (((ntg & 3) ^ xl) * 32) + p * 8; } }
#define G_LDA(dst, b, h) do { _Pragma("unroll") for (int m = 0; m < 4; ++m) { dst[m][0] = *(const LAS bf16x8*)(lds + G_SA(b, h) + aoff0 + m * 2048); \
        dst[m][1] = *(const LAS bf16x8*)(lds + G_SA(b, h) + aoff1 + m * 2048); } } while (0)
#define G_LDB(dst, b, h) do { _Pragma("unroll") for (int n = 0; n < 2; ++n) { \
        dst[n][0] = tr_read_a<G_SA(b, h)>(lbase + boff[n]); dst[n][1] = tr_read_a<G_SA(b, h) + 512>(lbase + boff[n]); \
        dst[n][2] = tr_read_a<G_SA(b, h) + 4096>(lbase + boff[n]); dst[n][3] = tr_read_a<G_SA(b, h) + 4608>(lbase + boff[n]); } } while (0)
#define G_WAITB(R) do { asm volatile("s_waitcnt lgkmcnt(0)" : "+v"(R[0][0]), "+v"(R[0][1]), "+v"(R[0][2]), "+v"(R[0][3]), "+v"(R[1][0]), "+v"(R[1][1]), "+v"(R[1][2]), "+v"(R[1][3]) :: "memory"); } while (0)
#define G_FR(R, n, k) __builtin_shufflevector(R[n][2 * (k)], R[n][2 * (k) + 1], 0, 1, 2, 3, 4, 5, 6, 7)
#define G_MMA(ai, bj, At, Bt) do { __builtin_amdgcn_s_setprio(1); _Pragma("unroll") for (int m = 0; m < 4; ++m) _Pragma("unroll") for (int n = 0; n < 2; ++n) \
        _Pragma("unroll") for (int k = 0; k < 2; ++k) acc[ai][bj][m][n] = __builtin_amdgcn_mfma_f32_16x16x32_bf16(G_FR(Bt, n, k), At[m][k], acc[ai][bj][m][n], 0, 0, 0); \
        __builtin_amdgcn_s_setprio(0); } while (0)
    f32x4 acc[2][2][4][2];
#pragma unroll
    for (int a = 0; a < 2; ++a)
#pragma unroll
        for (int b = 0; b < 2; ++b)
#pragma unroll
            for (int m = 0; m < 4; ++m)
#pragma unroll
                for (int n = 0; n < 2; ++n) acc[a][b][m][n] = (f32x4){0.f, 0.f, 0.f, 0.f};
    bf16x8 At[4][2]; s16x4 B0[2][4], B1[2][4];
    const unsigned lbase = (unsigned)(size_t)lds + 4 * HTB;
    G_STAGE(G_SB(0, 0), gB0, boffs); G_STAGE(G_SA(0, 0), gA0, aoffs); G_STAGE(G_SB(0, 1), gB1, boffs); G_STAGE(G_SA(0, 1), gA1, aoffs);
    if (wr == 1) BAR;
    WAIT_V(4); BAR;
    G_STAGE(G_SB(1, 0), gB0 + bkstep, boffs); G_STAGE(G_SA(1, 0), gA0 + akstep, aoffs); G_STAGE(G_SB(1, 1), gB1 + bkstep, boffs);
    WAIT_V(6); BAR;
    STAMP_AT();
    for (int t = 0; t < nt; t += 2) {
        const int t1 = t + 1, t2 = (t + 2 < nt) ? t + 2 : nt - 1, t3 = (t + 3 < nt) ? t + 3 : nt - 1;
        G_LDB(B0, 0, 0); SCHED; G_LDA(At, 0, 0); G_STAGE(G_SA(1, 1), gA1 + t1 * akstep, aoffs);
        WAIT_L(8); BAR; G_WAITB(B0); G_MMA(0, 0, At, B0); BAR; SCHED;
        G_LDB(B1, 0, 1); G_STAGE(G_SB(0, 0), gB0 + t2 * bkstep, boffs);
        BAR; G_WAITB(B1); G_MMA(0, 1, At, B1); BAR;
        G_LDA(At, 0, 1); G_STAGE(G_SA(0, 0), gA0 + t2 * akstep, aoffs);
        BAR; WAIT_L(0); G_MMA(1, 0, At, B0); BAR; SCHED;
        G_STAGE(G_SB(0, 1), gB1 + t2 * bkstep, boffs);
        WAIT_V(6); BAR; G_MMA(1, 1, At, B1); BAR;
        G_LDB(B0, 1, 0); SCHED; G_LDA(At, 1, 0); G_STAGE(G_SA(0, 1), gA1 + t2 * akstep, aoffs);
        WAIT_L(8); BAR; G_WAITB(B0); G_MMA(0, 0, At, B0); BAR; SCHED;
        G_LDB(B1, 1, 1); G_STAGE(G_SB(1, 0), gB0 + t3 * bkstep, boffs);
        BAR; G_WAITB(B1); G_MMA(0, 1, At, B1); BAR;
        G_LDA(At, 1, 1); G_STAGE(G_SA(1, 0), gA0 + t3 * akstep, aoffs);
        BAR; WAIT_L(0); G_MMA(1, 0, At, B0); BAR; SCHED;
        G_STAGE(G_SB(1, 1), gB1 + t3 * bkstep, boffs);
        WAIT_V(6); BAR; G_MMA(1, 1, At, B1); BAR;
    }
    STAMP_AT();
    WAIT_V(0);
    if (wr == 0) BAR;
#pragma unroll
    for (int ai = 0; ai < 2; ++ai)
#pragma unroll
        for (int m = 0; m < 4; ++m) {
            const int row = m0 + ai * 128 + wr * 64 + m * 16 + fr;
#pragma unroll
            for (int n = 0; n < 2; ++n) {
                const f32x4 g = acc[ai][0][m][n], u = acc[ai][1][m][n];
                float o[4];
#pragma unroll
                for (int j = 0; j < 4; ++j) o[j] = g[j] / (1.0f + __expf(-g[j])) * u[j];
                u32x2 w; w.x = pk_bf16(o[0], o[1]); w.y = pk_bf16(o[2], o[3]);
                *(u32x2*)(act + (size_t)row * F_ + f0 + wc * 32 + n * 16 + 4 * fq) = w;
            }
        }
    WAIT_V(0); STAMP_AT(); STAMP_RT2();
#undef G_SA
#undef G_SB
#undef G_STAGE
#undef G_LDA
#undef G_LDB
#undef G_MMA
#undef G_WAITB
#undef G_FR
}

constexpr int AT_ROWS = 320;
__global__ __launch_bounds__(256, 4) void attn_kernel(const bf16_t* __restrict__ qkv, bf16_t* __restrict__ o, const unsigned* __restrict__ pf0,
                                                   const unsigned* __restrict__ pf1, const unsigned* __restrict__ pf2) {
    __shared__ __attribute__((aligned(16))) unsigned char sm[2 * AT_ROWS * 64];
    LAS unsigned char* lk = (LAS unsigned char*)sm; LAS unsigned char* lv = lk + AT_ROWS * 64;
    const int tid = threadIdx.x, wid = __builtin_amdgcn_readfirstlane(tid >> 6), lane = tid & 63, fr = lane & 15, fq = lane >> 4;
    const int head = blockIdx.x & 31, qb = blockIdx.x >> 5, q0 = qb * 64;
    const bf16_t* Q = qkv + (size_t)head * S_ * 32;
    const bf16_t* Kg = qkv + (size_t)(32 + head) * S_ * 32;
    const bf16_t* Vg = qkv + (size_t)(64 + head) * S_ * 32;
    for (int pc = wid; pc < AT_ROWS / 16; pc += 4) {
        const int row = pc * 16 + (lane >> 2), slot = lane & 3;
        int key = q0 - 256 + row; key = key < 0 ? 0 : (key > S_ - 1 ? S_ - 1 : key);
        const int chk = slot ^ ((-(row >> 2)) & 3), chv = slot ^ (((row >> 2) & 1) << 1);
        glds16(Kg + (size_t)key * 32 + chk * 8, lk + pc * 1024);
        glds16(Vg + (size_t)key * 32 + chv * 8, lv + pc * 1024);
    }
    const int qrow = q0 + wid * 16 + fr;
    const bf16x8 qf = *(const bf16x8*)(Q + (size_t)qrow * 32 + fq * 8);
    WAIT_V(0);
    __syncthreads();
    unsigned pfv = 0;
    { const unsigned li = blockIdx.x * 256u + threadIdx.x; constexpr unsigned NL = (unsigned)((size_t)D_ * F_ * 2 / 128);
      if (li < 3 * NL) { const unsigned* pf = li < NL ? pf0 : (li < 2 * NL ? pf1 : pf2); const unsigned l2 = li < NL ? li : (li < 2 * NL ? li - NL : li - 2 * NL);
          pfv = pf[(size_t)l2 * 32]; } }
    const int kbase = q0 + wid * 16 - 256;
    const int istart = kbase < 0 ? (-kbase) >> 4 : 0;
    f32x4 s[17];
    const int koff = (wid * 16 + fr) * 64 + ((fq ^ ((-(fr >> 2)) & 3)) * 16);
#pragma unroll
    for (int i = 0; i < 17; ++i) {
        const bf16x8 kf = *(const LAS bf16x8*)(lk + koff + i * 1024);
        s[i] = __builtin_amdgcn_mfma_f32_16x16x32_bf16(kf, qf, (f32x4){0.f, 0.f, 0.f, 0.f}, 0, 0, 0);
    }
    const float NEG = -3.0e38f;
#pragma unroll
    for (int i = 0; i < 17; ++i) {
        const unsigned w0 = pk_bf16(s[i][0], s[i][1]), w1 = pk_bf16(s[i][2], s[i][3]);
        const float a0 = __uint_as_float(w0 << 16) * 0.17677669529663687f, a1 = __uint_as_float(w0 & 0xffff0000u) * 0.17677669529663687f;
        const float a2 = __uint_as_float(w1 << 16) * 0.17677669529663687f, a3 = __uint_as_float(w1 & 0xffff0000u) * 0.17677669529663687f;
        const unsigned z0 = pk_bf16(a0, a1), z1 = pk_bf16(a2, a3);
        s[i][0] = __uint_as_float(z0 << 16); s[i][1] = __uint_as_float(z0 & 0xffff0000u);
        s[i][2] = __uint_as_float(z1 << 16); s[i][3] = __uint_as_float(z1 & 0xffff0000u);
    }
#pragma unroll
    for (int r = 0; r < 4; ++r) { if (!(4 * fq + r > fr)) s[0][r] = NEG; if (!(4 * fq + r <= fr)) s[16][r] = NEG; }
#pragma unroll
    for (int i = 0; i < 16; ++i) if (i < istart) s[i] = (f32x4){NEG, NEG, NEG, NEG};
    float mx = NEG;
#pragma unroll
    for (int i = 0; i < 17; ++i) mx = fmaxf(mx, fmaxf(fmaxf(s[i][0], s[i][1]), fmaxf(s[i][2], s[i][3])));
    mx = fmaxf(mx, __shfl_xor(mx, 16)); mx = fmaxf(mx, __shfl_xor(mx, 32));
    const float c = 1.4426950408889634f;
    const float mc = mx * c;
    float sum = 0.f;
#pragma unroll
    for (int i = 0; i < 17; ++i)
#pragma unroll
        for (int r = 0; r < 4; ++r) { const float p = __builtin_amdgcn_exp2f(s[i][r] * c - mc); s[i][r] = p; sum += p; }
    sum += __shfl_xor(sum, 16); sum += __shfl_xor(sum, 32);
    const float inv = 1.0f / sum;
#pragma unroll
    for (int i = 0; i < 17; ++i) s[i] *= inv;
    f32x4 oacc[2] = {(f32x4){0.f, 0.f, 0.f, 0.f}, (f32x4){0.f, 0.f, 0.f, 0.f}};
    const int q4 = (lane & 15) >> 2, p4 = lane & 3;
    const int voff = (wid * 16 + 4 * fq + q4) * 64 + p4 * 8;
    const int vx = (fq & 1) << 5;
#pragma unroll
    for (int cc = 0; cc < 9; ++cc) {
        u32x4 pw; pw.x = pk_bf16(s[2 * cc][0], s[2 * cc][1]); pw.y = pk_bf16(s[2 * cc][2], s[2 * cc][3]);
        if (cc < 8) { pw.z = pk_bf16(s[2 * cc + 1][0], s[2 * cc + 1][1]); pw.w = pk_bf16(s[2 * cc + 1][2], s[2 * cc + 1][3]); } else { pw.z = 0u; pw.w = 0u; }
        const bf16x8 pf = __builtin_bit_cast(bf16x8, pw);
#pragma unroll
        for (int dt = 0; dt < 2; ++dt) {
            const s16x4 lo = tr_read(lv + voff + cc * 2048 + ((dt * 32) ^ vx)), hi = tr_read(lv + voff + cc * 2048 + (cc < 8 ? 1024 : 0) + ((dt * 32) ^ vx));
            const bf16x8 vf = __builtin_shufflevector(lo, hi, 0, 1, 2, 3, 4, 5, 6, 7);
            oacc[dt] = __builtin_amdgcn_mfma_f32_16x16x32_bf16(vf, pf, oacc[dt], 0, 0, 0);
        }
    }
#pragma unroll
    for (int dt = 0; dt < 2; ++dt) {
        u32x2 w; w.x = pk_bf16(oacc[dt][0], oacc[dt][1]); w.y = pk_bf16(oacc[dt][2], oacc[dt][3]);
        *(u32x2*)(o + (size_t)qrow * D_ + head * 32 + dt * 16 + 4 * fq) = w;
    }
    asm volatile("" :: "v"(pfv));
}

extern "C" void kernel_launch(void* const* d_in, const int* in_sizes, int n_in, void* d_out, int out_size, void* d_ws, size_t ws_size,
                              hipStream_t stream) {
    const bf16_t* x = (const bf16_t*)d_in[0];
    const bf16_t* wqkv = (const bf16_t*)d_in[1];
    const bf16_t* wo = (const bf16_t*)d_in[2];
    const bf16_t* w1 = (const bf16_t*)d_in[4];
    const bf16_t* w3 = (const bf16_t*)d_in[5];
    const bf16_t* w2 = (const bf16_t*)d_in[6];
    bf16_t* out = (bf16_t*)d_out;
    char* ws = (char*)d_ws;
    bf16_t* xn = (bf16_t*)(ws);
    bf16_t* qkv = (bf16_t*)(ws + (size_t)4 * 1024 * 1024);
    bf16_t* o = (bf16_t*)(ws + (size_t)20 * 1024 * 1024);
    bf16_t* h = (bf16_t*)(ws + (size_t)24 * 1024 * 1024);
    bf16_t* hn = (bf16_t*)(ws + (size_t)28 * 1024 * 1024);
    bf16_t* act = (bf16_t*)(ws + (size_t)32 * 1024 * 1024);

    ln_kernel<<<S_ / 4, 256, 0, stream>>>(x, xn, (const unsigned*)wqkv, (unsigned)((size_t)D_ * 3 * D_ * 2 / 128),
                                          (const unsigned*)wo, (unsigned)((size_t)D_ * D_ * 2 / 128));
    {
        auto kern = gemm_pp<128, 192, 2, 4, 3, EPI_QKV, 8, 4, 1024>;
        const int shm = 3 * (128 + 192) * 128;
        (void)hipFuncSetAttribute((const void*)kern, hipFuncAttributeMaxDynamicSharedMemorySize, shm);
        kern<<<256, 512, shm, stream>>>(xn, D_, wqkv, 3 * D_, nullptr, qkv, 16);
    }
    attn_kernel<<<32 * 32, 256, 0, stream>>>(qkv, o, (const unsigned*)w1, (const unsigned*)w3, (const unsigned*)w2);
    {
        auto kern = gemm_pp<128, 64, 4, 2, 6, EPI_RES, 4, 8, 1024>;
        const int shm = 6 * (128 + 64) * 128;
        (void)hipFuncSetAttribute((const void*)kern, hipFuncAttributeMaxDynamicSharedMemorySize, shm);
        kern<<<256, 512, shm, stream>>>(o, D_, wo, D_, x, h, 16);
    }
    ln_kernel<<<S_ / 4, 256, 0, stream>>>(h, hn, (const unsigned*)w2, 0u, (const unsigned*)w2, 0u);
    {
        const int shm = 8 * 16384;
        (void)hipFuncSetAttribute((const void*)gemm_gateup, hipFuncAttributeMaxDynamicSharedMemorySize, shm);
        gemm_gateup<<<224, 512, shm, stream>>>(hn, w1, w3, act);
    }
    {
        auto kern = gemm_pp<128, 64, 4, 2, 6, EPI_RES, 4, 8, 3584>;
        const int shm = 6 * (128 + 64) * 128;
        (void)hipFuncSetAttribute((const void*)kern, hipFuncAttributeMaxDynamicSharedMemorySize, shm);
        kern<<<256, 512, shm, stream>>>(act, F_, w2, D_, h, out, 16);
    }
}
```

```cpp
#include <hip/hip_runtime.h>
#include <hip/hip_bf16.h>
#include <stdint.h>

typedef unsigned short bf16_t;
typedef short bf16x8 __attribute__((ext_vector_type(8)));
typedef short s16x4 __attribute__((ext_vector_type(4)));
typedef float f32x4 __attribute__((ext_vector_type(4)));
typedef unsigned u32x4 __attribute__((ext_vector_type(4)));
typedef unsigned u32x2 __attribute__((ext_vector_type(2)));
#define LAS __attribute__((address_space(3)))
#define DI __device__ __forceinline__

constexpr int S_ = 2048, D_ = 1024, F_ = 3584, H_ = 32, DH_ = 32, WIN_ = 256;

DI float bf2f(unsigned short b) { return __uint_as_float(((unsigned)b) << 16); }
DI unsigned pk_bf16(float lo, float hi) {
    typedef __bf16 bf2 __attribute__((ext_vector_type(2)));
    bf2 v; v.x = (__bf16)lo; v.y = (__bf16)hi;
    return __builtin_bit_cast(unsigned, v);
}
DI s16x4 tr_read(LAS unsigned char* p) { return __builtin_amdgcn_ds_read_tr16_b64_v4i16((LAS s16x4*)p); }
template <int OFF> DI s16x4 tr_read_a(unsigned addr) { s16x4 r; asm volatile("ds_read_b64_tr_b16 %0, %1 offset:%2" : "=v"(r) : "v"(addr), "i"(OFF)); return r; }
DI void glds16(const void* g, LAS unsigned char* l) {
    __builtin_amdgcn_global_load_lds((const __attribute__((address_space(1))) unsigned*)g, (LAS unsigned*)l, 16, 0, 0);
}
#define WAIT_V(n) asm volatile("s_waitcnt vmcnt(" #n ")" ::: "memory")
#define WAIT_L(n) asm volatile("s_waitcnt lgkmcnt(" #n ")" ::: "memory")
#define BAR __builtin_amdgcn_s_barrier()
#define SCHED __builtin_amdgcn_sched_barrier(0)
#define STAMP_DECL(kid)
#define STAMP_AT()
#define STAMP_RT()
#define STAMP_RT2()

__global__ __launch_bounds__(256) void ln_kernel(const bf16_t* __restrict__ in, bf16_t* __restrict__ out, const unsigned* __restrict__ pf, unsigned pf_lines,
                                                 const unsigned* __restrict__ pfb, unsigned pfb_lines) {
    const int row = blockIdx.x * 4 + (threadIdx.x >> 6), lane = threadIdx.x & 63;
    const u32x4* p = (const u32x4*)(in + (size_t)row * D_);
    u32x4 a = p[lane], b = p[lane + 64];
    unsigned pfv = 0;
    { const unsigned li = blockIdx.x * 256u + threadIdx.x; if (li < pf_lines) pfv = pf[(size_t)li * 32];
      else if (li - pf_lines < pfb_lines) pfv = pfb[(size_t)(li - pf_lines) * 32]; }
    float v[16];
#pragma unroll
    for (int i = 0; i < 4; ++i) { v[2 * i] = __uint_as_float(a[i] << 16); v[2 * i + 1] = __uint_as_float(a[i] & 0xffff0000u);
                                  v[8 + 2 * i] = __uint_as_float(b[i] << 16); v[8 + 2 * i + 1] = __uint_as_float(b[i] & 0xffff0000u); }
    float s = 0.f;
#pragma unroll
    for (int i = 0; i < 16; ++i) s += v[i];
#pragma unroll
    for (int o = 32; o >= 1; o >>= 1) s += __shfl_xor(s, o);
    const float mean = s * (1.0f / D_);
    float q = 0.f;
#pragma unroll
    for (int i = 0; i < 16; ++i) { v[i] -= mean; q += v[i] * v[i]; }
#pragma unroll
    for (int o = 32; o >= 1; o >>= 1) q += __shfl_xor(q, o);
    const float rstd = 1.0f / sqrtf(q * (1.0f / D_) + 1e-5f);
    u32x4 oa, ob;
#pragma unroll
    for (int i = 0; i < 4; ++i) { oa[i] = pk_bf16(v[2 * i] * rstd, v[2 * i + 1] * rstd); ob[i] = pk_bf16(v[8 + 2 * i] * rstd, v[8 + 2 * i + 1] * rstd); }
    u32x4* po = (u32x4*)(out + (size_t)row * D_);
    po[lane] = oa; po[lane + 64] = ob;
    asm volatile("" :: "v"(pfv));
}

DI int bx_of_k(int k) { return ((k >> 1) & 1) | (((k >> 3) & 1) << 1); }

enum { EPI_QKV = 0, EPI_RES = 1 };
#define QKV_LNF 0
DI u32x4 pair_swap(u32x2 t0, u32x2 t1) {
    auto a = __builtin_amdgcn_permlane16_swap(t0.x, t1.x, false, false);
    auto b = __builtin_amdgcn_permlane16_swap(t0.y, t1.y, false, false);
    return (u32x4){a[0], b[0], a[1], b[1]};
}

template <int N> DI void wait_v() { asm volatile("s_waitcnt vmcnt(%0)" :: "i"(N) : "memory"); }
template <int OFF> DI bf16x8 ld128_a(unsigned addr) { bf16x8 r; asm volatile("ds_read_b128 %0, %1 offset:%2" : "=v"(r) : "v"(addr), "i"(OFF)); return r; }
typedef __bf16 bf2_t __attribute__((ext_vector_type(2)));
DI float dot2_one(unsigned w, float c) { return __builtin_amdgcn_fdot2_f32_bf16(__builtin_bit_cast(bf2_t, w), __builtin_bit_cast(bf2_t, 0x3F803F80u), c, false); }
DI float dot2_sq(unsigned w, float c) { return __builtin_amdgcn_fdot2_f32_bf16(__builtin_bit_cast(bf2_t, w), __builtin_bit_cast(bf2_t, w), c, false); }
DI void row_stats_acc(const bf16x8& f, float& rs, float& rq) { const u32x4 w = __builtin_bit_cast(u32x4, f);
#pragma unroll
    for (int i = 0; i < 4; ++i) { rs = dot2_one(w[i], rs); rq = dot2_sq(w[i], rq); } }
DI void col_sum_acc(const s16x4& lo, const s16x4& hi, float& cs) { const u32x2 a = __builtin_bit_cast(u32x2, lo), b = __builtin_bit_cast(u32x2, hi);
    cs = dot2_one(a.x, cs); cs = dot2_one(a.y, cs); cs = dot2_one(b.x, cs); cs = dot2_one(b.y, cs); }
DI float red_fq(float v) { v += __shfl_xor(v, 16); v += __shfl_xor(v, 32); return v; }

template <int BM, int BN, int WM, int WN, int NBUF, int EPI, int RM, int RN, int K>
__global__ __launch_bounds__(512, 2) void gemm_pp(const bf16_t* __restrict__ A, int lda, const bf16_t* __restrict__ W, int ldb,
                                                   const bf16_t* __restrict__ res, bf16_t* __restrict__ out, int tiles_m) {
    extern __shared__ __attribute__((aligned(16))) unsigned char smem_raw[];
    LAS unsigned char* lds = (LAS unsigned char*)smem_raw;
    constexpr int MT = BM / WM / 16, NT = BN / WN / 16;
    constexpr int NPA = BM / 64, NPB = BN / 64, NP = NPA + NPB;
    constexpr int ABYTES = BM * 128, TILE_BYTES = (BM + BN) * 128;
    constexpr int nt = K / 64;
    constexpr bool LNF = (EPI == EPI_QKV) && QKV_LNF;
    static_assert(WM * WN == 8, "8 waves");
    static_assert(MT == 2 || MT == 4, "MT");
    static_assert(!LNF || (MT == 4 && WN == 4 && WM == 2), "LN fold geometry");
    STAMP_DECL(EPI == EPI_QKV ? 0 : (K == 1024 ? 1 : 2)); STAMP_RT(); STAMP_AT();
    const int tid = threadIdx.x, wid = __builtin_amdgcn_readfirstlane(tid >> 6), lane = tid & 63;
    const int wm = wid / WN, wn = wid % WN, fr = lane & 15, fq = lane >> 4;
    int tm, tn;
    { const int bid = blockIdx.x, xcd = bid & 7, idx = bid >> 3; const int regm = tiles_m / RM;
      const int rr = xcd % regm, rc = xcd / regm; tm = rr * RM + (idx % RM); tn = rc * RN + (idx / RM); }
    const int m0 = tm * BM, n0 = tn * BN;
    const bf16_t* asrc[NPA]; const bf16_t* bsrc[NPB];
#pragma unroll
    for (int i = 0; i < NPA; ++i) { const int row = (wid + 8 * i) * 8 + (lane >> 3), ch = (lane & 7) ^ ((row >> 1) & 7);
        asrc[i] = A + (size_t)(m0 + row) * lda + ch * 8; }
#pragma unroll
    for (int j = 0; j < NPB; ++j) { const int k = wid * 8 + (lane >> 3), s16 = lane & 7, n16 = (s16 >> 1) ^ bx_of_k(k);
        bsrc[j] = W + (size_t)k * ldb + n0 + j * 64 + n16 * 16 + (s16 & 1) * 8; }
    const size_t bstep = (size_t)64 * ldb;
    auto stage = [&](int t, int buf) {
        LAS unsigned char* base = lds + buf * TILE_BYTES + wid * 1024;
#pragma unroll
        for (int i = 0; i < NPA; ++i) glds16(asrc[i] + (size_t)t * 64, base + i * 8192);
#pragma unroll
        for (int j = 0; j < NPB; ++j) glds16(bsrc[j] + (size_t)t * bstep, base + ABYTES + j * 8192);
    };
    const int rotm = LNF ? wn : 0, krot = LNF ? wm : 0;
    const int sw = (fr >> 1) & 7;
    unsigned aadr[MT][2], badr[NT][2];
#pragma unroll
    for (int j = 0; j < MT; ++j)
#pragma unroll
        for (int sl = 0; sl < 2; ++sl) aadr[j][sl] = (unsigned)(size_t)lds + (wm * MT * 16 + ((j + rotm) & (MT - 1)) * 16 + fr) * 128 + (((((sl ^ krot) * 4) + fq) ^ sw) * 16);
    { const int q = (lane & 15) >> 2, p = lane & 3, xl = ((q >> 1) & 1) | ((fq & 1) << 1);
#pragma unroll
      for (int n = 0; n < NT; ++n) { const int ntg = wn * NT + n;
          const unsigned b0 = (unsigned)(size_t)lds + ABYTES + (ntg >> 2) * 8192 + (8 * fq + q) * 128 + (((ntg & 3) ^ xl) * 32) + p * 8;
          badr[n][0] = b0 + krot * 4096; badr[n][1] = b0 + (1 - krot) * 4096; } }
    f32x4 acc[MT][NT];
#pragma unroll
    for (int m = 0; m < MT; ++m)
#pragma unroll
        for (int n = 0; n < NT; ++n) acc[m][n] = (f32x4){0.f, 0.f, 0.f, 0.f};
    float rs = 0.f, rq = 0.f, cs[NT];
#pragma unroll
    for (int n = 0; n < NT; ++n) cs[n] = 0.f;
    bf16x8 afA[MT][2], afB[MT][2]; s16x4 bA[NT][4], bB[NT][4];
#define P_READ(AF, BR, buf) do { const unsigned bo_ = (unsigned)((buf) * TILE_BYTES); \
        if constexpr (LNF) { \
            _Pragma("unroll") for (int n = 0; n < NT; ++n) { const unsigned b0_ = badr[n][0] + bo_, b1_ = badr[n][1] + bo_; \
                BR[n][0] = tr_read_a<0>(b0_); BR[n][1] = tr_read_a<512>(b0_); BR[n][2] = tr_read_a<0>(b1_); BR[n][3] = tr_read_a<512>(b1_); } \
            _Pragma("unroll") for (int j = 0; j < MT; ++j) { AF[j][0] = ld128_a<0>(aadr[j][0] + bo_); AF[j][1] = ld128_a<0>(aadr[j][1] + bo_); } \
        } else { \
            _Pragma("unroll") for (int n = 0; n < NT; ++n) { const unsigned ba = badr[n][0] + bo_; \
                BR[n][0] = tr_read_a<0>(ba); BR[n][1] = tr_read_a<512>(ba); BR[n][2] = tr_read_a<4096>(ba); BR[n][3] = tr_read_a<4608>(ba); } \
            const unsigned a0_ = aadr[0][0] + bo_, a1_ = aadr[0][1] + bo_; \
            AF[0][0] = ld128_a<0>(a0_); AF[0][1] = ld128_a<0>(a1_); AF[1][0] = ld128_a<2048>(a0_); AF[1][1] = ld128_a<2048>(a1_); \
            if constexpr (MT == 4) { AF[MT - 2][0] = ld128_a<4096>(a0_); AF[MT - 2][1] = ld128_a<4096>(a1_); AF[MT - 1][0] = ld128_a<6144>(a0_); AF[MT - 1][1] = ld128_a<6144>(a1_); } } } while (0)
#define P_WAITL(AF, BR) do { _Pragma("unroll") for (int n = 0; n < NT; ++n) \
        asm volatile("s_waitcnt lgkmcnt(0)" : "+v"(BR[n][0]), "+v"(BR[n][1]), "+v"(BR[n][2]), "+v"(BR[n][3]) :: "memory"); \
        _Pragma("unroll") for (int m = 0; m < MT; ++m) asm volatile("" : "+v"(AF[m][0]), "+v"(AF[m][1])); } while (0)
#define P_MMA(AF, BR) do { _Pragma("unroll") for (int kk = 0; kk < 2; ++kk) _Pragma("unroll") for (int m = 0; m < MT; ++m) _Pragma("unroll") for (int n = 0; n < NT; ++n) \
        acc[m][n] = __builtin_amdgcn_mfma_f32_16x16x32_bf16(__builtin_shufflevector(BR[n][2 * kk], BR[n][2 * kk + 1], 0, 1, 2, 3, 4, 5, 6, 7), AF[m][kk], acc[m][n], 0, 0, 0); \
        if constexpr (LNF) { row_stats_acc(AF[0][0], rs, rq); row_stats_acc(AF[0][1], rs, rq); \
            _Pragma("unroll") for (int n = 0; n < NT; ++n) col_sum_acc(BR[n][0], BR[n][1], cs[n]); } } while (0)
#define P_STEP(AFC, BRC, AFN, BRN, FULL, WN_, RESLD) do { \
        P_WAITL(AFC, BRC); \
        wait_v<WN_>(); \
        BAR; \
        const int nb_ = (buf + 1 == NBUF) ? 0 : buf + 1; \
        if (FULL || t + 1 < nt) P_READ(AFN, BRN, nb_); \
        if (FULL || t + NBUF < nt) stage(t + NBUF, buf); \
        if (RESLD) { _Pragma("unroll") for (int m = 0; m < MT; ++m) _Pragma("unroll") for (int n = 0; n < NT; ++n) \
            rres[m][n] = *(const u32x2*)(res + (size_t)(m0 + (wm * MT + m) * 16 + fr) * D_ + n0 + (wn * NT + n) * 16 + 4 * fq); } \
        P_MMA(AFC, BRC); \
        buf = nb_; ++t; } while (0)
    u32x2 rres[MT][NT];
    constexpr int NR = (EPI == EPI_RES) ? MT * NT : 0;
#pragma unroll
    for (int t = 0; t < NBUF; ++t) stage(t, t);
    wait_v<(NBUF - 1) * NP>();
    BAR;
    STAMP_AT();
    P_READ(afA, bA, 0);
    int buf = 0, t = 0;
    constexpr int nmain = ((nt - NBUF) / 2) * 2;
    for (; t < nmain;) {
        P_STEP(afA, bA, afB, bB, true, (NBUF - 2) * NP, false);
        P_STEP(afB, bB, afA, bA, true, (NBUF - 2) * NP, false);
    }
#define P_TAILW(tt) ((((tt) + NBUF - 1 < nt - 1 ? (tt) + NBUF - 1 : nt - 1) - ((tt) + 1) > 0 ? (((tt) + NBUF - 1 < nt - 1 ? (tt) + NBUF - 1 : nt - 1) - ((tt) + 1)) : 0) * NP + ((tt) > nt - NBUF ? NR : 0))
#define P_TAIL2(tt) do { t = (tt); P_STEP(afA, bA, afB, bB, false, P_TAILW(tt), (NR > 0 && (tt) == nt - NBUF)); \
        P_STEP(afB, bB, afA, bA, false, P_TAILW((tt) + 1), (NR > 0 && (tt) + 1 == nt - NBUF)); } while (0)
    if constexpr (nmain + 0 < nt) P_TAIL2(nmain + 0);
    if constexpr (nmain + 2 < nt) P_TAIL2(nmain + 2);
    if constexpr (nmain + 4 < nt) P_TAIL2(nmain + 4);
    if constexpr (nmain + 6 < nt) P_TAIL2(nmain + 6);
    static_assert(nmain + 8 >= nt, "tail");
#undef P_TAILW
#undef P_TAIL2
    STAMP_AT();
#undef P_READ
#undef P_WAITL
#undef P_MMA
#undef P_STEP
    if constexpr (EPI == EPI_RES) {
#pragma unroll
        for (int m = 0; m < MT; ++m)
#pragma unroll
            for (int n = 0; n < NT; ++n) asm volatile("" : "+v"(rres[m][n]));
    }
    typedef float f32x2 __attribute__((ext_vector_type(2)));
    LAS f32x2* stat = (LAS f32x2*)lds;
    LAS float* cpart = (LAS float*)(lds + BM * 8);
    if constexpr (LNF) {
        rs = red_fq(rs); rq = red_fq(rq);
        const float mean = rs * (1.0f / K), var = fmaxf(rq * (1.0f / K) - mean * mean, 0.f);
        if (fq == 0) stat[wm * 64 + wn * 16 + fr] = (f32x2){mean, 1.0f / sqrtf(var + 1e-5f)};
#pragma unroll
        for (int n = 0; n < NT; ++n) { const float c = red_fq(cs[n]); if (fq == 0) cpart[wm * BN + (wn * NT + n) * 16 + fr] = c; }
        __syncthreads();
    }
    unsigned pfv = 0;
    if constexpr (EPI == EPI_QKV) { const unsigned li = blockIdx.x * 512u + threadIdx.x; if (li < (unsigned)((size_t)D_ * D_ * 2 / 128)) pfv = ((const unsigned*)res)[(size_t)li * 32]; }
    const int pcol = (fq & 1) ? 16 + 4 * (fq - 1) : 4 * fq;
    f32x4 csum[NT];
    if constexpr (LNF) {
#pragma unroll
        for (int n = 0; n < NT; ++n) csum[n] = *(const LAS f32x4*)(cpart + (wn * NT + n) * 16 + 4 * fq) + *(const LAS f32x4*)(cpart + BN + (wn * NT + n) * 16 + 4 * fq);
    }
#pragma unroll
    for (int m = 0; m < MT; ++m) {
        const int lrow = (wm * MT + ((m + rotm) & (MT - 1))) * 16 + fr, row = m0 + lrow;
        f32x2 st; if constexpr (LNF) st = stat[lrow];
        u32x2 w[NT];
#pragma unroll
        for (int n = 0; n < NT; ++n) {
            f32x4 v = acc[m][n];
            if constexpr (LNF) v = (v - csum[n] * st.x) * st.y;
            if constexpr (EPI == EPI_RES) { const u32x2 r = rres[m][n];
                v[0] += __uint_as_float(r.x << 16); v[1] += __uint_as_float(r.x & 0xffff0000u);
                v[2] += __uint_as_float(r.y << 16); v[3] += __uint_as_float(r.y & 0xffff0000u); }
            w[n].x = pk_bf16(v[0], v[1]); w[n].y = pk_bf16(v[2], v[3]);
        }
#pragma unroll
        for (int n = 0; n + 1 < NT; n += 2) {
            const u32x4 o = pair_swap(w[n], w[n + 1]);
            const int col = n0 + (wn * NT + n) * 16 + pcol;
            if constexpr (EPI == EPI_QKV) *(u32x4*)(out + ((size_t)(col >> 5) * S_ + row) * 32 + (col & 31)) = o;
            else *(u32x4*)(out + (size_t)row * D_ + col) = o;
        }
        if constexpr (NT & 1) {
            const int col = n0 + (wn * NT + NT - 1) * 16 + 4 * fq;
            if constexpr (EPI == EPI_QKV) *(u32x2*)(out + ((size_t)(col >> 5) * S_ + row) * 32 + (col & 31)) = w[NT - 1];
            else *(u32x2*)(out + (size_t)row * D_ + col) = w[NT - 1];
        }
    }
    asm volatile("" :: "v"(pfv));
    WAIT_V(0); STAMP_AT(); STAMP_RT2();
}

__global__ __launch_bounds__(512, 2) void gemm_gateup(const bf16_t* __restrict__ A, const bf16_t* __restrict__ W1, const bf16_t* __restrict__ W3,
                                                       bf16_t* __restrict__ act, const unsigned* __restrict__ pf) {
    extern __shared__ __attribute__((aligned(16))) unsigned char smem_raw[];
    LAS unsigned char* lds = (LAS unsigned char*)smem_raw;
    constexpr int K = D_, HTB = 16384;
    STAMP_DECL(3); STAMP_RT(); STAMP_AT();
    const int tid = threadIdx.x, wid = __builtin_amdgcn_readfirstlane(tid >> 6), lane = tid & 63;
    const int wr = wid >> 2, wc = wid & 3, fr = lane & 15, fq = lane >> 4;
    int tm, tn;
    { const int bid = blockIdx.x, xcd = bid & 7, idx = bid >> 3; tm = (xcd & 1) * 4 + (idx & 3); tn = (xcd >> 1) * 7 + (idx >> 2); }
    const int m0 = tm * 256, f0 = tn * 128;
    constexpr int nt = K / 64;
    unsigned aoffs[2], boffs[2];
#pragma unroll
    for (int i = 0; i < 2; ++i) { const int row = (wid + 8 * i) * 8 + (lane >> 3), ch = (lane & 7) ^ ((row >> 1) & 7);
        aoffs[i] = (unsigned)(row * K + ch * 8) * 2u;
        const int k = wid * 8 + (lane >> 3), s16 = lane & 7, n16 = (s16 >> 1) ^ bx_of_k(k);
        boffs[i] = (unsigned)(k * F_ + i * 64 + n16 * 16 + (s16 & 1) * 8) * 2u; }
    const char* gA0 = (const char*)(A + (size_t)m0 * K); const char* gA1 = gA0 + (size_t)128 * K * 2;
    const char* gB0 = (const char*)(W1 + f0); const char* gB1 = (const char*)(W3 + f0);
    constexpr size_t akstep = 128, bkstep = (size_t)64 * F_ * 2;
    const unsigned ldsw = (unsigned)wid * 1024u;
#define G_SA(b, h) (((b) * 2 + (h)) * HTB)
#define G_SB(b, h) ((4 + (b) * 2 + (h)) * HTB)
#define G_STAGE(bufoff, gbase, voff) do { _Pragma("unroll") for (int _i = 0; _i < 2; ++_i) glds16((gbase) + (voff)[_i], lds + (bufoff) + ldsw + _i * 8192); } while (0)
    const int sw = (fr >> 1) & 7;
    const int aoff0 = (wr * 64 + fr) * 128 + (((wr * 4 + fq) ^ sw) * 16), aoff1 = aoff0 ^ 64;
    unsigned badr[2][2];
    { const int q = (lane & 15) >> 2, p = lane & 3, xl = ((q >> 1) & 1) | ((fq & 1) << 1);
#pragma unroll
      for (int n = 0; n < 2; ++n) { const int ntg = wc * 2 + n;
          const unsigned b0 = (unsigned)(size_t)lds + 4 * HTB + (ntg >> 2) * 8192 + (8 * fq + q) * 128 + (((ntg & 3) ^ xl) * 32) + p * 8;
          badr[n][0] = b0 + wr * 4096; badr[n][1] = b0 + (1 - wr) * 4096; } }
#define G_LDA(dst, b, h) do { _Pragma("unroll") for (int m = 0; m < 4; ++m) { dst[m][0] = *(const LAS bf16x8*)(lds + G_SA(b, h) + aoff0 + m * 2048); \
        dst[m][1] = *(const LAS bf16x8*)(lds + G_SA(b, h) + aoff1 + m * 2048); } } while (0)
#define G_LDB(dst, b, h) do { _Pragma("unroll") for (int n = 0; n < 2; ++n) { \
        dst[n][0] = tr_read_a<G_SA(b, h)>(badr[n][0]); dst[n][1] = tr_read_a<G_SA(b, h) + 512>(badr[n][0]); \
        dst[n][2] = tr_read_a<G_SA(b, h)>(badr[n][1]); dst[n][3] = tr_read_a<G_SA(b, h) + 512>(badr[n][1]); } } while (0)
#define G_WAITB(R) do { asm volatile("s_waitcnt lgkmcnt(0)" : "+v"(R[0][0]), "+v"(R[0][1]), "+v"(R[0][2]), "+v"(R[0][3]), "+v"(R[1][0]), "+v"(R[1][1]), "+v"(R[1][2]), "+v"(R[1][3]) :: "memory"); } while (0)
#define G_FR(R, n, k) __builtin_shufflevector(R[n][2 * (k)], R[n][2 * (k) + 1], 0, 1, 2, 3, 4, 5, 6, 7)
#define G_MMA(ai, bj, At, Bt) do { __builtin_amdgcn_s_setprio(1); _Pragma("unroll") for (int m = 0; m < 4; ++m) _Pragma("unroll") for (int n = 0; n < 2; ++n) \
        _Pragma("unroll") for (int k = 0; k < 2; ++k) acc[ai][bj][m][n] = __builtin_amdgcn_mfma_f32_16x16x32_bf16(G_FR(Bt, n, k), At[m][k], acc[ai][bj][m][n], 0, 0, 0); \
        __builtin_amdgcn_s_setprio(0); } while (0)
    f32x4 acc[2][2][4][2];
#pragma unroll
    for (int a = 0; a < 2; ++a)
#pragma unroll
        for (int b = 0; b < 2; ++b)
#pragma unroll
            for (int m = 0; m < 4; ++m)
#pragma unroll
                for (int n = 0; n < 2; ++n) acc[a][b][m][n] = (f32x4){0.f, 0.f, 0.f, 0.f};
    bf16x8 At[4][2]; s16x4 B0[2][4], B1[2][4];
    G_STAGE(G_SB(0, 0), gB0, boffs); G_STAGE(G_SA(0, 0), gA0, aoffs); G_STAGE(G_SB(0, 1), gB1, boffs); G_STAGE(G_SA(0, 1), gA1, aoffs);
    if (wr == 1) BAR;
    WAIT_V(4); BAR;
    G_STAGE(G_SB(1, 0), gB0 + bkstep, boffs); G_STAGE(G_SA(1, 0), gA0 + akstep, aoffs); G_STAGE(G_SB(1, 1), gB1 + bkstep, boffs);
    WAIT_V(6); BAR;
    STAMP_AT();
    for (int t = 0; t < nt; t += 2) {
        const int t1 = t + 1, t2 = (t + 2 < nt) ? t + 2 : nt - 1, t3 = (t + 3 < nt) ? t + 3 : nt - 1;
        G_LDB(B0, 0, 0); SCHED; G_LDA(At, 0, 0); G_STAGE(G_SA(1, 1), gA1 + t1 * akstep, aoffs);
        WAIT_L(8); BAR; G_WAITB(B0); G_MMA(0, 0, At, B0); BAR; SCHED;
        G_LDB(B1, 0, 1); G_STAGE(G_SB(0, 0), gB0 + t2 * bkstep, boffs);
        BAR; G_WAITB(B1); G_MMA(0, 1, At, B1); BAR;
        G_LDA(At, 0, 1); G_STAGE(G_SA(0, 0), gA0 + t2 * akstep, aoffs);
        BAR; WAIT_L(0); G_MMA(1, 0, At, B0); BAR; SCHED;
        G_STAGE(G_SB(0, 1), gB1 + t2 * bkstep, boffs);
        WAIT_V(6); BAR; G_MMA(1, 1, At, B1); BAR;
        G_LDB(B0, 1, 0); SCHED; G_LDA(At, 1, 0); G_STAGE(G_SA(0, 1), gA1 + t2 * akstep, aoffs);
        WAIT_L(8); BAR; G_WAITB(B0); G_MMA(0, 0, At, B0); BAR; SCHED;
        G_LDB(B1, 1, 1); G_STAGE(G_SB(1, 0), gB0 + t3 * bkstep, boffs);
        BAR; G_WAITB(B1); G_MMA(0, 1, At, B1); BAR;
        G_LDA(At, 1, 1); G_STAGE(G_SA(1, 0), gA0 + t3 * akstep, aoffs);
        BAR; WAIT_L(0); G_MMA(1, 0, At, B0); BAR; SCHED;
        G_STAGE(G_SB(1, 1), gB1 + t3 * bkstep, boffs);
        WAIT_V(6); BAR; G_MMA(1, 1, At, B1); BAR;
    }
    STAMP_AT();
    WAIT_V(0);
    if (wr == 0) BAR;
    unsigned pfv = 0;
    { const unsigned li = blockIdx.x * 512u + threadIdx.x; if (li < (unsigned)((size_t)F_ * D_ * 2 / 128)) pfv = pf[(size_t)li * 32]; }
    const int pcol = (fq & 1) ? 16 + 4 * (fq - 1) : 4 * fq;
#pragma unroll
    for (int ai = 0; ai < 2; ++ai)
#pragma unroll
        for (int m = 0; m < 4; ++m) {
            const int lrow = ai * 128 + wr * 64 + m * 16 + fr, row = m0 + lrow;
            u32x2 w[2];
#pragma unroll
            for (int n = 0; n < 2; ++n) {
                const f32x4 g = acc[ai][0][m][n], u = acc[ai][1][m][n];
                float o[4];
#pragma unroll
                for (int j = 0; j < 4; ++j) o[j] = g[j] * __builtin_amdgcn_rcpf(1.0f + __builtin_amdgcn_exp2f(g[j] * -1.4426950408889634f)) * u[j];
                w[n].x = pk_bf16(o[0], o[1]); w[n].y = pk_bf16(o[2], o[3]);
            }
            *(u32x4*)(act + (size_t)row * F_ + f0 + wc * 32 + pcol) = pair_swap(w[0], w[1]);
        }
    asm volatile("" :: "v"(pfv));
    WAIT_V(0); STAMP_AT(); STAMP_RT2();
#undef G_SA
#undef G_SB
#undef G_STAGE
#undef G_LDA
#undef G_LDB
#undef G_MMA
#undef G_WAITB
#undef G_FR
}

constexpr int AT_QB = 256, AT_ROWS = AT_QB + 256, AT_NW = AT_QB / 16;
__global__ __launch_bounds__(AT_NW * 64, 4) void attn_kernel(const bf16_t* __restrict__ qkv, bf16_t* __restrict__ o) {
    __shared__ __attribute__((aligned(16))) unsigned char sm[2 * AT_ROWS * 64];
    LAS unsigned char* lk = (LAS unsigned char*)sm; LAS unsigned char* lv = lk + AT_ROWS * 64;
    const int tid = threadIdx.x, wid = __builtin_amdgcn_readfirstlane(tid >> 6), lane = tid & 63, fr = lane & 15, fq = lane >> 4;
    const int head = blockIdx.x & 31, qb = blockIdx.x >> 5, q0 = qb * AT_QB;
    const bf16_t* Q = qkv + (size_t)head * S_ * 32;
    const bf16_t* Kg = qkv + (size_t)(32 + head) * S_ * 32;
    const bf16_t* Vg = qkv + (size_t)(64 + head) * S_ * 32;
#pragma unroll
    for (int pc = wid; pc < AT_ROWS / 16; pc += AT_NW) {
        const int row = pc * 16 + (lane >> 2), slot = lane & 3;
        int key = q0 - 256 + row; key = key < 0 ? 0 : (key > S_ - 1 ? S_ - 1 : key);
        const int chk = slot ^ ((-(row >> 2)) & 3), chv = slot ^ (((row >> 2) & 1) << 1);
        glds16(Kg + (size_t)key * 32 + chk * 8, lk + pc * 1024);
        glds16(Vg + (size_t)key * 32 + chv * 8, lv + pc * 1024);
    }
    const int qrow = q0 + wid * 16 + fr;
    bf16x8 qf;
    asm volatile("global_load_dwordx4 %0, %1, off" : "=v"(qf) : "v"(Q + (size_t)qrow * 32 + fq * 8));
    asm volatile("s_waitcnt vmcnt(0)" : "+v"(qf) :: "memory");
    __builtin_amdgcn_s_barrier();
    const int kbase = q0 + wid * 16 - 256;
    const int istart = kbase < 0 ? (-kbase) >> 4 : 0;
    f32x4 s[17];
    const int koff = (wid * 16 + fr) * 64 + ((fq ^ ((-(fr >> 2)) & 3)) * 16);
#pragma unroll
    for (int i = 0; i < 17; ++i) {
        const bf16x8 kf = *(const LAS bf16x8*)(lk + koff + i * 1024);
        s[i] = __builtin_amdgcn_mfma_f32_16x16x32_bf16(kf, qf, (f32x4){0.f, 0.f, 0.f, 0.f}, 0, 0, 0);
    }
    asm volatile("s_nop 7\n\ts_nop 7\n\ts_nop 3" : "+v"(s[0]), "+v"(s[1]), "+v"(s[2]), "+v"(s[3]), "+v"(s[4]), "+v"(s[5]), "+v"(s[6]), "+v"(s[7]), "+v"(s[8]),
                 "+v"(s[9]), "+v"(s[10]), "+v"(s[11]), "+v"(s[12]), "+v"(s[13]), "+v"(s[14]), "+v"(s[15]), "+v"(s[16]));
    const float NEG = -3.0e38f;
#pragma unroll
    for (int r = 0; r < 4; ++r) { if (!(4 * fq + r > fr)) s[0][r] = NEG; if (!(4 * fq + r <= fr)) s[16][r] = NEG; }
    if (istart > 0) {
#pragma unroll
        for (int i = 0; i < 16; ++i) if (i < istart) s[i] = (f32x4){NEG, NEG, NEG, NEG};
    }
    float mx = NEG;
#pragma unroll
    for (int i = 0; i < 17; ++i) { asm("v_max3_f32 %0, %1, %2, %3" : "=v"(mx) : "v"(mx), "v"(s[i][0]), "v"(s[i][1]));
                                   asm("v_max3_f32 %0, %1, %2, %3" : "=v"(mx) : "v"(mx), "v"(s[i][2]), "v"(s[i][3])); }
    mx = fmaxf(mx, __shfl_xor(mx, 16)); mx = fmaxf(mx, __shfl_xor(mx, 32));
    const float c = 0.17677669529663687f * 1.4426950408889634f;
    const float mc = -mx * c;
    typedef float f32x2 __attribute__((ext_vector_type(2)));
    const f32x2 c2 = {c, c}, mc2 = {mc, mc};
#pragma unroll
    for (int i = 0; i < 17; ++i) {
        const f32x2 lo = __builtin_elementwise_fma((f32x2){s[i][0], s[i][1]}, c2, mc2), hi = __builtin_elementwise_fma((f32x2){s[i][2], s[i][3]}, c2, mc2);
        s[i][0] = __builtin_amdgcn_exp2f(lo.x); s[i][1] = __builtin_amdgcn_exp2f(lo.y); s[i][2] = __builtin_amdgcn_exp2f(hi.x); s[i][3] = __builtin_amdgcn_exp2f(hi.y);
    }
    f32x4 oacc[2] = {(f32x4){0.f, 0.f, 0.f, 0.f}, (f32x4){0.f, 0.f, 0.f, 0.f}};
    f32x4 osum = (f32x4){0.f, 0.f, 0.f, 0.f};
    const bf16x8 ones = (bf16x8){0x3F80, 0x3F80, 0x3F80, 0x3F80, 0x3F80, 0x3F80, 0x3F80, 0x3F80};
    const int q4 = (lane & 15) >> 2, p4 = lane & 3;
    const unsigned va0 = (unsigned)(size_t)lv + (wid * 16 + 4 * fq + q4) * 64 + p4 * 8 + ((fq & 1) << 5), va1 = va0 ^ 32;
    s16x4 vr[2][4];
#define V_READ(SET, cc) do { vr[SET][0] = tr_read_a<(cc) * 2048>(va0); vr[SET][1] = tr_read_a<(cc) * 2048 + ((cc) < 8 ? 1024 : 0)>(va0); \
        vr[SET][2] = tr_read_a<(cc) * 2048>(va1); vr[SET][3] = tr_read_a<(cc) * 2048 + ((cc) < 8 ? 1024 : 0)>(va1); } while (0)
#define V_STEP(cc) do { constexpr int cur = (cc) & 1; \
        if constexpr ((cc) < 8) { V_READ(cur ^ 1, (cc) + 1); asm volatile("s_waitcnt lgkmcnt(4)" : "+v"(vr[cur][0]), "+v"(vr[cur][1]), "+v"(vr[cur][2]), "+v"(vr[cur][3])); } \
        else asm volatile("s_waitcnt lgkmcnt(0)" : "+v"(vr[cur][0]), "+v"(vr[cur][1]), "+v"(vr[cur][2]), "+v"(vr[cur][3])); \
        u32x4 pw; pw.x = pk_bf16(s[2 * (cc)][0], s[2 * (cc)][1]); pw.y = pk_bf16(s[2 * (cc)][2], s[2 * (cc)][3]); \
        if constexpr ((cc) < 8) { pw.z = pk_bf16(s[2 * (cc) + 1][0], s[2 * (cc) + 1][1]); pw.w = pk_bf16(s[2 * (cc) + 1][2], s[2 * (cc) + 1][3]); } else { pw.z = 0u; pw.w = 0u; } \
        const bf16x8 pf = __builtin_bit_cast(bf16x8, pw); \
        osum = __builtin_amdgcn_mfma_f32_16x16x32_bf16(ones, pf, osum, 0, 0, 0); \
        oacc[0] = __builtin_amdgcn_mfma_f32_16x16x32_bf16(__builtin_shufflevector(vr[cur][0], vr[cur][1], 0, 1, 2, 3, 4, 5, 6, 7), pf, oacc[0], 0, 0, 0); \
        oacc[1] = __builtin_amdgcn_mfma_f32_16x16x32_bf16(__builtin_shufflevector(vr[cur][2], vr[cur][3], 0, 1, 2, 3, 4, 5, 6, 7), pf, oacc[1], 0, 0, 0); } while (0)
    V_READ(0, 0);
    V_STEP(0); V_STEP(1); V_STEP(2); V_STEP(3); V_STEP(4); V_STEP(5); V_STEP(6); V_STEP(7); V_STEP(8);
#undef V_READ
#undef V_STEP
    const float inv = 1.0f / osum[0];
#pragma unroll
    for (int dt = 0; dt < 2; ++dt) {
        u32x2 w; w.x = pk_bf16(oacc[dt][0] * inv, oacc[dt][1] * inv); w.y = pk_bf16(oacc[dt][2] * inv, oacc[dt][3] * inv);
        *(u32x2*)(o + (size_t)qrow * D_ + head * 32 + dt * 16 + 4 * fq) = w;
    }
}

extern "C" void kernel_launch(void* const* d_in, const int* in_sizes, int n_in, void* d_out, int out_size, void* d_ws, size_t ws_size,
                              hipStream_t stream) {
    const bf16_t* x = (const bf16_t*)d_in[0];
    const bf16_t* wqkv = (const bf16_t*)d_in[1];
    const bf16_t* wo = (const bf16_t*)d_in[2];
    const bf16_t* w1 = (const bf16_t*)d_in[4];
    const bf16_t* w3 = (const bf16_t*)d_in[5];
    const bf16_t* w2 = (const bf16_t*)d_in[6];
    bf16_t* out = (bf16_t*)d_out;
    char* ws = (char*)d_ws;
    bf16_t* xn = (bf16_t*)(ws);
    bf16_t* qkv = (bf16_t*)(ws + (size_t)4 * 1024 * 1024);
    bf16_t* o = (bf16_t*)(ws + (size_t)20 * 1024 * 1024);
    bf16_t* h = (bf16_t*)(ws + (size_t)24 * 1024 * 1024);
    bf16_t* hn = (bf16_t*)(ws + (size_t)28 * 1024 * 1024);
    bf16_t* act = (bf16_t*)(ws + (size_t)32 * 1024 * 1024);

    ln_kernel<<<S_ / 4, 256, 0, stream>>>(x, xn, (const unsigned*)wqkv, 0u, (const unsigned*)wo, 0u);
    {
        auto kern = gemm_pp<128, 192, 2, 4, 3, EPI_QKV, 8, 4, 1024>;
        const int shm = 3 * (128 + 192) * 128;
        (void)hipFuncSetAttribute((const void*)kern, hipFuncAttributeMaxDynamicSharedMemorySize, shm);
        kern<<<256, 512, shm, stream>>>(QKV_LNF ? x : xn, D_, wqkv, 3 * D_, wo, qkv, 16);
    }
    attn_kernel<<<32 * (S_ / AT_QB), AT_NW * 64, 0, stream>>>(qkv, o);
    {
        auto kern = gemm_pp<128, 64, 4, 2, 6, EPI_RES, 4, 8, 1024>;
        const int shm = 6 * (128 + 64) * 128;
        (void)hipFuncSetAttribute((const void*)kern, hipFuncAttributeMaxDynamicSharedMemorySize, shm);
        kern<<<256, 512, shm, stream>>>(o, D_, wo, D_, x, h, 16);
    }
    ln_kernel<<<S_ / 4, 256, 0, stream>>>(h, hn, (const unsigned*)w2, 0u, (const unsigned*)w2, 0u);
    {
        const int shm = 8 * 16384;
        (void)hipFuncSetAttribute((const void*)gemm_gateup, hipFuncAttributeMaxDynamicSharedMemorySize, shm);
        gemm_gateup<<<224, 512, shm, stream>>>(hn, w1, w3, act, (const unsigned*)w2);
    }
    {
        auto kern = gemm_pp<128, 64, 4, 2, 6, EPI_RES, 4, 8, 3584>;
        const int shm = 6 * (128 + 64) * 128;
        (void)hipFuncSetAttribute((const void*)kern, hipFuncAttributeMaxDynamicSharedMemorySize, shm);
        kern<<<256, 512, shm, stream>>>(act, F_, w2, D_, h, out, 16);
    }
}
```

```cpp
#include <hip/hip_runtime.h>
#include <hip/hip_bf16.h>
#include <stdint.h>

typedef unsigned short bf16_t;
typedef short bf16x8 __attribute__((ext_vector_type(8)));
typedef short s16x4 __attribute__((ext_vector_type(4)));
typedef float f32x4 __attribute__((ext_vector_type(4)));
typedef unsigned u32x4 __attribute__((ext_vector_type(4)));
typedef unsigned u32x2 __attribute__((ext_vector_type(2)));
#define LAS __attribute__((address_space(3)))
#define DI __device__ __forceinline__

constexpr int S_ = 2048, D_ = 1024, F_ = 3584, H_ = 32, DH_ = 32, WIN_ = 256;

DI float bf2f(unsigned short b) { return __uint_as_float(((unsigned)b) << 16); }
DI unsigned pk_bf16(float lo, float hi) {
    typedef __bf16 bf2 __attribute__((ext_vector_type(2)));
    bf2 v; v.x = (__bf16)lo; v.y = (__bf16)hi;
    return __builtin_bit_cast(unsigned, v);
}
DI s16x4 tr_read(LAS unsigned char* p) { return __builtin_amdgcn_ds_read_tr16_b64_v4i16((LAS s16x4*)p); }
template <int OFF> DI s16x4 tr_read_a(unsigned addr) { s16x4 r; asm volatile("ds_read_b64_tr_b16 %0, %1 offset:%2" : "=v"(r) : "v"(addr), "i"(OFF)); return r; }
DI void glds16(const void* g, LAS unsigned char* l) {
    __builtin_amdgcn_global_load_lds((const __attribute__((address_space(1))) unsigned*)g, (LAS unsigned*)l, 16, 0, 0);
}
#define WAIT_V(n) asm volatile("s_waitcnt vmcnt(" #n ")" ::: "memory")
#define WAIT_L(n) asm volatile("s_waitcnt lgkmcnt(" #n ")" ::: "memory")
#define BAR __builtin_amdgcn_s_barrier()
#define SCHED __builtin_amdgcn_sched_barrier(0)
#define STAMP_DECL(kid)
#define STAMP_AT()
#define STAMP_RT()
#define STAMP_RT2()

__global__ __launch_bounds__(256) void ln_kernel(const bf16_t* __restrict__ in, bf16_t* __restrict__ out, const unsigned* __restrict__ pf, unsigned pf_lines,
                                                 const unsigned* __restrict__ pfb, unsigned pfb_lines) {
    const int row = blockIdx.x * 4 + (threadIdx.x >> 6), lane = threadIdx.x & 63;
    const u32x4* p = (const u32x4*)(in + (size_t)row * D_);
    u32x4 a = p[lane], b = p[lane + 64];
    unsigned pfv = 0;
    { const unsigned li = blockIdx.x * 256u + threadIdx.x; if (li < pf_lines) pfv = pf[(size_t)li * 32];
      else if (li - pf_lines < pfb_lines) pfv = pfb[(size_t)(li - pf_lines) * 32]; }
    float v[16];
#pragma unroll
    for (int i = 0; i < 4; ++i) { v[2 * i] = __uint_as_float(a[i] << 16); v[2 * i + 1] = __uint_as_float(a[i] & 0xffff0000u);
                                  v[8 + 2 * i] = __uint_as_float(b[i] << 16); v[8 + 2 * i + 1] = __uint_as_float(b[i] & 0xffff0000u); }
    float s = 0.f;
#pragma unroll
    for (int i = 0; i < 16; ++i) s += v[i];
#pragma unroll
    for (int o = 32; o >= 1; o >>= 1) s += __shfl_xor(s, o);
    const float mean = s * (1.0f / D_);
    float q = 0.f;
#pragma unroll
    for (int i = 0; i < 16; ++i) { v[i] -= mean; q += v[i] * v[i]; }
#pragma unroll
    for (int o = 32; o >= 1; o >>= 1) q += __shfl_xor(q, o);
    const float rstd = 1.0f / sqrtf(q * (1.0f / D_) + 1e-5f);
    u32x4 oa, ob;
#pragma unroll
    for (int i = 0; i < 4; ++i) { oa[i] = pk_bf16(v[2 * i] * rstd, v[2 * i + 1] * rstd); ob[i] = pk_bf16(v[8 + 2 * i] * rstd, v[8 + 2 * i + 1] * rstd); }
    u32x4* po = (u32x4*)(out + (size_t)row * D_);
    po[lane] = oa; po[lane + 64] = ob;
    asm volatile("" :: "v"(pfv));
}

DI int bx_of_k(int k) { return ((k >> 1) & 1) | (((k >> 3) & 1) << 1); }

enum { EPI_QKV = 0, EPI_RES = 1 };
#define QKV_LNF 1
DI u32x4 pair_swap(u32x2 t0, u32x2 t1) {
    auto a = __builtin_amdgcn_permlane16_swap(t0.x, t1.x, false, false);
    auto b = __builtin_amdgcn_permlane16_swap(t0.y, t1.y, false, false);
    return (u32x4){a[0], b[0], a[1], b[1]};
}

template <int N> DI void wait_v() { asm volatile("s_waitcnt vmcnt(%0)" :: "i"(N) : "memory"); }
template <int OFF> DI bf16x8 ld128_a(unsigned addr) { bf16x8 r; asm volatile("ds_read_b128 %0, %1 offset:%2" : "=v"(r) : "v"(addr), "i"(OFF)); return r; }
typedef __bf16 bf2_t __attribute__((ext_vector_type(2)));
DI float dot2_one(unsigned w, float c) { return __builtin_amdgcn_fdot2_f32_bf16(__builtin_bit_cast(bf2_t, w), __builtin_bit_cast(bf2_t, 0x3F803F80u), c, false); }
DI float dot2_sq(unsigned w, float c) { return __builtin_amdgcn_fdot2_f32_bf16(__builtin_bit_cast(bf2_t, w), __builtin_bit_cast(bf2_t, w), c, false); }
DI void row_stats_acc(const bf16x8& f, float& rs, float& rq) { const u32x4 w = __builtin_bit_cast(u32x4, f);
#pragma unroll
    for (int i = 0; i < 4; ++i) { rs = dot2_one(w[i], rs); rq = dot2_sq(w[i], rq); } }
DI void col_sum_acc(const s16x4& lo, const s16x4& hi, float& cs) { const u32x2 a = __builtin_bit_cast(u32x2, lo), b = __builtin_bit_cast(u32x2, hi);
    cs = dot2_one(a.x, cs); cs = dot2_one(a.y, cs); cs = dot2_one(b.x, cs); cs = dot2_one(b.y, cs); }
DI float red_fq(float v) { v += __shfl_xor(v, 16); v += __shfl_xor(v, 32); return v; }

template <int BM, int BN, int WM, int WN, int NBUF, int EPI, int RM, int RN, int K>
__global__ __launch_bounds__(512, 2) void gemm_pp(const bf16_t* __restrict__ A, int lda, const bf16_t* __restrict__ W, int ldb,
                                                   const bf16_t* __restrict__ res, bf16_t* __restrict__ out, int tiles_m) {
    extern __shared__ __attribute__((aligned(16))) unsigned char smem_raw[];
    LAS unsigned char* lds = (LAS unsigned char*)smem_raw;
    constexpr int MT = BM / WM / 16, NT = BN / WN / 16;
    constexpr int NPA = BM / 64, NPB = BN / 64, NP = NPA + NPB;
    constexpr int ABYTES = BM * 128, TILE_BYTES = (BM + BN) * 128;
    constexpr int nt = K / 64;
    constexpr bool LNF = (EPI == EPI_QKV) && QKV_LNF;
    static_assert(WM * WN == 8, "8 waves");
    static_assert(MT == 2 || MT == 4, "MT");
    static_assert(!LNF || (MT == 4 && WN == 4 && WM == 2), "LN fold geometry");
    STAMP_DECL(EPI == EPI_QKV ? 0 : (K == 1024 ? 1 : 2)); STAMP_RT(); STAMP_AT();
    const int tid = threadIdx.x, wid = __builtin_amdgcn_readfirstlane(tid >> 6), lane = tid & 63;
    const int wm = wid / WN, wn = wid % WN, fr = lane & 15, fq = lane >> 4;
    int tm, tn;
    { const int bid = blockIdx.x, xcd = bid & 7, idx = bid >> 3; const int regm = tiles_m / RM;
      const int rr = xcd % regm, rc = xcd / regm; tm = rr * RM + (idx % RM); tn = rc * RN + (idx / RM); }
    const int m0 = tm * BM, n0 = tn * BN;
    const bf16_t* asrc[NPA]; const bf16_t* bsrc[NPB];
#pragma unroll
    for (int i = 0; i < NPA; ++i) { const int row = (wid + 8 * i) * 8 + (lane >> 3), ch = (lane & 7) ^ ((row >> 1) & 7);
        asrc[i] = A + (size_t)(m0 + row) * lda + ch * 8; }
#pragma unroll
    for (int j = 0; j < NPB; ++j) { const int k = wid * 8 + (lane >> 3), s16 = lane & 7, n16 = (s16 >> 1) ^ bx_of_k(k);
        bsrc[j] = W + (size_t)k * ldb + n0 + j * 64 + n16 * 16 + (s16 & 1) * 8; }
    const size_t bstep = (size_t)64 * ldb;
    auto stage = [&](int t, int buf) {
        LAS unsigned char* base = lds + buf * TILE_BYTES + wid * 1024;
#pragma unroll
        for (int i = 0; i < NPA; ++i) glds16(asrc[i] + (size_t)t * 64, base + i * 8192);
#pragma unroll
        for (int j = 0; j < NPB; ++j) glds16(bsrc[j] + (size_t)t * bstep, base + ABYTES + j * 8192);
    };
    const int rotm = LNF ? wn : 0, krot = LNF ? wm : 0;
    const int sw = (fr >> 1) & 7;
    unsigned aadr[MT][2], badr[NT][2];
#pragma unroll
    for (int j = 0; j < MT; ++j)
#pragma unroll
        for (int sl = 0; sl < 2; ++sl) aadr[j][sl] = (unsigned)(size_t)lds + (wm * MT * 16 + ((j + rotm) & (MT - 1)) * 16 + fr) * 128 + (((((sl ^ krot) * 4) + fq) ^ sw) * 16);
    { const int q = (lane & 15) >> 2, p = lane & 3, xl = ((q >> 1) & 1) | ((fq & 1) << 1);
#pragma unroll
      for (int n = 0; n < NT; ++n) { const int ntg = wn * NT + n;
          const unsigned b0 = (unsigned)(size_t)lds + ABYTES + (ntg >> 2) * 8192 + (8 * fq + q) * 128 + (((ntg & 3) ^ xl) * 32) + p * 8;
          badr[n][0] = b0 + krot * 4096; badr[n][1] = b0 + (1 - krot) * 4096; } }
    f32x4 acc[MT][NT];
#pragma unroll
    for (int m = 0; m < MT; ++m)
#pragma unroll
        for (int n = 0; n < NT; ++n) acc[m][n] = (f32x4){0.f, 0.f, 0.f, 0.f};
    f32x4 rsacc = (f32x4){0.f, 0.f, 0.f, 0.f}, rqacc = rsacc, csacc[NT];
#pragma unroll
    for (int n = 0; n < NT; ++n) csacc[n] = rsacc;
    const bf16x8 ones = (bf16x8){0x3F80, 0x3F80, 0x3F80, 0x3F80, 0x3F80, 0x3F80, 0x3F80, 0x3F80};
    bf16x8 afA[MT][2], afB[MT][2]; s16x4 bA[NT][4], bB[NT][4];
#define P_READ(AF, BR, buf) do { const unsigned bo_ = (unsigned)((buf) * TILE_BYTES); \
        if constexpr (LNF) { \
            _Pragma("unroll") for (int n = 0; n < NT; ++n) { const unsigned b0_ = badr[n][0] + bo_, b1_ = badr[n][1] + bo_; \
                BR[n][0] = tr_read_a<0>(b0_); BR[n][1] = tr_read_a<512>(b0_); BR[n][2] = tr_read_a<0>(b1_); BR[n][3] = tr_read_a<512>(b1_); } \
            _Pragma("unroll") for (int j = 0; j < MT; ++j) { AF[j][0] = ld128_a<0>(aadr[j][0] + bo_); AF[j][1] = ld128_a<0>(aadr[j][1] + bo_); } \
        } else { \
            _Pragma("unroll") for (int n = 0; n < NT; ++n) { const unsigned ba = badr[n][0] + bo_; \
                BR[n][0] = tr_read_a<0>(ba); BR[n][1] = tr_read_a<512>(ba); BR[n][2] = tr_read_a<4096>(ba); BR[n][3] = tr_read_a<4608>(ba); } \
            const unsigned a0_ = aadr[0][0] + bo_, a1_ = aadr[0][1] + bo_; \
            AF[0][0] = ld128_a<0>(a0_); AF[0][1] = ld128_a<0>(a1_); AF[1][0] = ld128_a<2048>(a0_); AF[1][1] = ld128_a<2048>(a1_); \
            if constexpr (MT == 4) { AF[MT - 2][0] = ld128_a<4096>(a0_); AF[MT - 2][1] = ld128_a<4096>(a1_); AF[MT - 1][0] = ld128_a<6144>(a0_); AF[MT - 1][1] = ld128_a<6144>(a1_); } } } while (0)
#define P_WAITL(AF, BR) do { _Pragma("unroll") for (int n = 0; n < NT; ++n) \
        asm volatile("s_waitcnt lgkmcnt(0)" : "+v"(BR[n][0]), "+v"(BR[n][1]), "+v"(BR[n][2]), "+v"(BR[n][3]) :: "memory"); \
        _Pragma("unroll") for (int m = 0; m < MT; ++m) asm volatile("" : "+v"(AF[m][0]), "+v"(AF[m][1])); } while (0)
#define P_MMA(AF, BR) do { _Pragma("unroll") for (int kk = 0; kk < 2; ++kk) _Pragma("unroll") for (int m = 0; m < MT; ++m) _Pragma("unroll") for (int n = 0; n < NT; ++n) \
        acc[m][n] = __builtin_amdgcn_mfma_f32_16x16x32_bf16(__builtin_shufflevector(BR[n][2 * kk], BR[n][2 * kk + 1], 0, 1, 2, 3, 4, 5, 6, 7), AF[m][kk], acc[m][n], 0, 0, 0); \
        if constexpr (LNF) { _Pragma("unroll") for (int kk = 0; kk < 2; ++kk) { rsacc = __builtin_amdgcn_mfma_f32_16x16x32_bf16(ones, AF[0][kk], rsacc, 0, 0, 0); \
                rqacc = __builtin_amdgcn_mfma_f32_16x16x32_bf16(AF[0][kk], AF[0][kk], rqacc, 0, 0, 0); } \
            _Pragma("unroll") for (int n = 0; n < NT; ++n) csacc[n] = __builtin_amdgcn_mfma_f32_16x16x32_bf16(__builtin_shufflevector(BR[n][0], BR[n][1], 0, 1, 2, 3, 4, 5, 6, 7), ones, csacc[n], 0, 0, 0); } } while (0)
#define P_STEP(AFC, BRC, AFN, BRN, FULL, WN_, RESLD) do { \
        P_WAITL(AFC, BRC); \
        wait_v<WN_>(); \
        BAR; \
        const int nb_ = (buf + 1 == NBUF) ? 0 : buf + 1; \
        if (FULL || t + 1 < nt) P_READ(AFN, BRN, nb_); \
        if (FULL || t + NBUF < nt) stage(t + NBUF, buf); \
        if (RESLD) { _Pragma("unroll") for (int m = 0; m < MT; ++m) _Pragma("unroll") for (int n = 0; n < NT; ++n) \
            rres[m][n] = *(const u32x2*)(res + (size_t)(m0 + (wm * MT + m) * 16 + fr) * D_ + n0 + (wn * NT + n) * 16 + 4 * fq); } \
        P_MMA(AFC, BRC); \
        buf = nb_; ++t; } while (0)
    u32x2 rres[MT][NT];
    constexpr int NR = (EPI == EPI_RES) ? MT * NT : 0;
#pragma unroll
    for (int t = 0; t < NBUF; ++t) stage(t, t);
    wait_v<(NBUF - 1) * NP>();
    BAR;
    STAMP_AT();
    P_READ(afA, bA, 0);
    int buf = 0, t = 0;
    constexpr int nmain = ((nt - NBUF) / 2) * 2;
    for (; t < nmain;) {
        P_STEP(afA, bA, afB, bB, true, (NBUF - 2) * NP, false);
        P_STEP(afB, bB, afA, bA, true, (NBUF - 2) * NP, false);
    }
#define P_TAILW(tt) ((((tt) + NBUF - 1 < nt - 1 ? (tt) + NBUF - 1 : nt - 1) - ((tt) + 1) > 0 ? (((tt) + NBUF - 1 < nt - 1 ? (tt) + NBUF - 1 : nt - 1) - ((tt) + 1)) : 0) * NP + ((tt) > nt - NBUF ? NR : 0))
#define P_TAIL2(tt) do { t = (tt); P_STEP(afA, bA, afB, bB, false, P_TAILW(tt), (NR > 0 && (tt) == nt - NBUF)); \
        P_STEP(afB, bB, afA, bA, false, P_TAILW((tt) + 1), (NR > 0 && (tt) + 1 == nt - NBUF)); } while (0)
    if constexpr (nmain + 0 < nt) P_TAIL2(nmain + 0);
    if constexpr (nmain + 2 < nt) P_TAIL2(nmain + 2);
    if constexpr (nmain + 4 < nt) P_TAIL2(nmain + 4);
    if constexpr (nmain + 6 < nt) P_TAIL2(nmain + 6);
    static_assert(nmain + 8 >= nt, "tail");
#undef P_TAILW
#undef P_TAIL2
    STAMP_AT();
#undef P_READ
#undef P_WAITL
#undef P_MMA
#undef P_STEP
    if constexpr (EPI == EPI_RES) {
#pragma unroll
        for (int m = 0; m < MT; ++m)
#pragma unroll
            for (int n = 0; n < NT; ++n) asm volatile("" : "+v"(rres[m][n]));
    }
    typedef float f32x2 __attribute__((ext_vector_type(2)));
    LAS f32x2* stat = (LAS f32x2*)lds;
    LAS float* cpart = (LAS float*)(lds + BM * 8);
    if constexpr (LNF) {
        const float rq = (fr & 2) ? ((fr & 1) ? rqacc[3] : rqacc[2]) : ((fr & 1) ? rqacc[1] : rqacc[0]);
        const float mean = rsacc[0] * (1.0f / K), var = fmaxf(rq * (1.0f / K) - mean * mean, 0.f);
        if (fq == (fr >> 2)) stat[wm * 64 + wn * 16 + fr] = (f32x2){mean, 1.0f / sqrtf(var + 1e-5f)};
#pragma unroll
        for (int n = 0; n < NT; ++n) if (fr == 0) *(LAS f32x4*)(cpart + wm * BN + (wn * NT + n) * 16 + 4 * fq) = csacc[n];
        __syncthreads();
    }
    unsigned pfv = 0;
    if constexpr (EPI == EPI_QKV) { const unsigned li = blockIdx.x * 512u + threadIdx.x; if (li < (unsigned)((size_t)D_ * D_ * 2 / 128)) pfv = ((const unsigned*)res)[(size_t)li * 32]; }
    const int pcol = (fq & 1) ? 16 + 4 * (fq - 1) : 4 * fq;
    f32x4 csum[NT];
    if constexpr (LNF) {
#pragma unroll
        for (int n = 0; n < NT; ++n) csum[n] = *(const LAS f32x4*)(cpart + (wn * NT + n) * 16 + 4 * fq) + *(const LAS f32x4*)(cpart + BN + (wn * NT + n) * 16 + 4 * fq);
    }
#pragma unroll
    for (int m = 0; m < MT; ++m) {
        const int lrow = (wm * MT + ((m + rotm) & (MT - 1))) * 16 + fr, row = m0 + lrow;
        f32x2 st; if constexpr (LNF) st = stat[lrow];
        u32x2 w[NT];
#pragma unroll
        for (int n = 0; n < NT; ++n) {
            f32x4 v = acc[m][n];
            if constexpr (LNF) v = (v - csum[n] * st.x) * st.y;
            if constexpr (EPI == EPI_RES) { const u32x2 r = rres[m][n];
                v[0] += __uint_as_float(r.x << 16); v[1] += __uint_as_float(r.x & 0xffff0000u);
                v[2] += __uint_as_float(r.y << 16); v[3] += __uint_as_float(r.y & 0xffff0000u); }
            w[n].x = pk_bf16(v[0], v[1]); w[n].y = pk_bf16(v[2], v[3]);
        }
#pragma unroll
        for (int n = 0; n + 1 < NT; n += 2) {
            const u32x4 o = pair_swap(w[n], w[n + 1]);
            const int col = n0 + (wn * NT + n) * 16 + pcol;
            if constexpr (EPI == EPI_QKV) *(u32x4*)(out + ((size_t)(col >> 5) * S_ + row) * 32 + (col & 31)) = o;
            else *(u32x4*)(out + (size_t)row * D_ + col) = o;
        }
        if constexpr (NT & 1) {
            const int col = n0 + (wn * NT + NT - 1) * 16 + 4 * fq;
            if constexpr (EPI == EPI_QKV) *(u32x2*)(out + ((size_t)(col >> 5) * S_ + row) * 32 + (col & 31)) = w[NT - 1];
            else *(u32x2*)(out + (size_t)row * D_ + col) = w[NT - 1];
        }
    }
    asm volatile("" :: "v"(pfv));
    WAIT_V(0); STAMP_AT(); STAMP_RT2();
}

__global__ __launch_bounds__(512, 2) void gemm_gateup(const bf16_t* __restrict__ A, const bf16_t* __restrict__ W1, const bf16_t* __restrict__ W3,
                                                       bf16_t* __restrict__ act, const unsigned* __restrict__ pf) {
    extern __shared__ __attribute__((aligned(16))) unsigned char smem_raw[];
    LAS unsigned char* lds = (LAS unsigned char*)smem_raw;
    constexpr int K = D_, HTB = 16384;
    STAMP_DECL(3); STAMP_RT(); STAMP_AT();
    const int tid = threadIdx.x, wid = __builtin_amdgcn_readfirstlane(tid >> 6), lane = tid & 63;
    const int wr = wid >> 2, wc = wid & 3, fr = lane & 15, fq = lane >> 4;
    int tm, tn;
    { const int bid = blockIdx.x, xcd = bid & 7, idx = bid >> 3; tm = (xcd & 1) * 4 + (idx & 3); tn = (xcd >> 1) * 7 + (idx >> 2); }
    const int m0 = tm * 256, f0 = tn * 128;
    constexpr int nt = K / 64;
    unsigned aoffs[2], boffs[2];
#pragma unroll
    for (int i = 0; i < 2; ++i) { const int row = (wid + 8 * i) * 8 + (lane >> 3), ch = (lane & 7) ^ ((row >> 1) & 7);
        aoffs[i] = (unsigned)(row * K + ch * 8) * 2u;
        const int k = wid * 8 + (lane >> 3), s16 = lane & 7, n16 = (s16 >> 1) ^ bx_of_k(k);
        boffs[i] = (unsigned)(k * F_ + i * 64 + n16 * 16 + (s16 & 1) * 8) * 2u; }
    const char* gA0 = (const char*)(A + (size_t)m0 * K); const char* gA1 = gA0 + (size_t)128 * K * 2;
    const char* gB0 = (const char*)(W1 + f0); const char* gB1 = (const char*)(W3 + f0);
    constexpr size_t akstep = 128, bkstep = (size_t)64 * F_ * 2;
    const unsigned ldsw = (unsigned)wid * 1024u;
#define G_SA(b, h) (((b) * 2 + (h)) * HTB)
#define G_SB(b, h) ((4 + (b) * 2 + (h)) * HTB)
#define G_STAGE(bufoff, gbase, voff) do { _Pragma("unroll") for (int _i = 0; _i < 2; ++_i) glds16((gbase) + (voff)[_i], lds + (bufoff) + ldsw + _i * 8192); } while (0)
    const int sw = (fr >> 1) & 7;
    const int aoff0 = (wr * 64 + fr) * 128 + (((wr * 4 + fq) ^ sw) * 16), aoff1 = aoff0 ^ 64;
    unsigned badr[2][2];
    { const int q = (lane & 15) >> 2, p = lane & 3, xl = ((q >> 1) & 1) | ((fq & 1) << 1);
#pragma unroll
      for (int n = 0; n < 2; ++n) { const int ntg = wc * 2 + n;
          const unsigned b0 = (unsigned)(size_t)lds + 4 * HTB + (ntg >> 2) * 8192 + (8 * fq + q) * 128 + (((ntg & 3) ^ xl) * 32) + p * 8;
          badr[n][0] = b0 + wr * 4096; badr[n][1] = b0 + (1 - wr) * 4096; } }
#define G_LDA(dst, b, h) do { _Pragma("unroll") for (int m = 0; m < 4; ++m) { dst[m][0] = *(const LAS bf16x8*)(lds + G_SA(b, h) + aoff0 + m * 2048); \
        dst[m][1] = *(const LAS bf16x8*)(lds + G_SA(b, h) + aoff1 + m * 2048); } } while (0)
#define G_LDB(dst, bo, h) do { _Pragma("unroll") for (int n = 0; n < 2; ++n) { const unsigned b0_ = badr[n][0] + (bo), b1_ = badr[n][1] + (bo); \
        dst[n][0] = tr_read_a<(h) * HTB>(b0_); dst[n][1] = tr_read_a<(h) * HTB + 512>(b0_); \
        dst[n][2] = tr_read_a<(h) * HTB>(b1_); dst[n][3] = tr_read_a<(h) * HTB + 512>(b1_); } } while (0)
#define G_WAITB(R) do { asm volatile("s_waitcnt lgkmcnt(0)" : "+v"(R[0][0]), "+v"(R[0][1]), "+v"(R[0][2]), "+v"(R[0][3]), "+v"(R[1][0]), "+v"(R[1][1]), "+v"(R[1][2]), "+v"(R[1][3]) :: "memory"); } while (0)
#define G_FR(R, n, k) __builtin_shufflevector(R[n][2 * (k)], R[n][2 * (k) + 1], 0, 1, 2, 3, 4, 5, 6, 7)
#define G_MMA(ai, bj, At, Bt) do { __builtin_amdgcn_s_setprio(1); _Pragma("unroll") for (int m = 0; m < 4; ++m) _Pragma("unroll") for (int n = 0; n < 2; ++n) \
        _Pragma("unroll") for (int k = 0; k < 2; ++k) acc[ai][bj][m][n] = __builtin_amdgcn_mfma_f32_16x16x32_bf16(G_FR(Bt, n, k), At[m][k], acc[ai][bj][m][n], 0, 0, 0); \
        __builtin_amdgcn_s_setprio(0); } while (0)
    f32x4 acc[2][2][4][2];
#pragma unroll
    for (int a = 0; a < 2; ++a)
#pragma unroll
        for (int b = 0; b < 2; ++b)
#pragma unroll
            for (int m = 0; m < 4; ++m)
#pragma unroll
                for (int n = 0; n < 2; ++n) acc[a][b][m][n] = (f32x4){0.f, 0.f, 0.f, 0.f};
    bf16x8 At[4][2]; s16x4 B0[2][4], B1[2][4];
    G_STAGE(G_SB(0, 0), gB0, boffs); G_STAGE(G_SA(0, 0), gA0, aoffs); G_STAGE(G_SB(0, 1), gB1, boffs); G_STAGE(G_SA(0, 1), gA1, aoffs);
    if (wr == 1) BAR;
    WAIT_V(4); BAR;
    G_STAGE(G_SB(1, 0), gB0 + bkstep, boffs); G_STAGE(G_SA(1, 0), gA0 + akstep, aoffs); G_STAGE(G_SB(1, 1), gB1 + bkstep, boffs);
    WAIT_V(6); BAR;
    STAMP_AT();
    for (int t = 0; t < nt; t += 2) {
        const int t1 = t + 1, t2 = (t + 2 < nt) ? t + 2 : nt - 1, t3 = (t + 3 < nt) ? t + 3 : nt - 1;
        G_LDB(B0, 0, 0); SCHED; G_LDA(At, 0, 0); G_STAGE(G_SA(1, 1), gA1 + t1 * akstep, aoffs);
        WAIT_L(8); BAR; G_WAITB(B0); G_MMA(0, 0, At, B0); BAR; SCHED;
        G_LDB(B1, 0, 1); G_STAGE(G_SB(0, 0), gB0 + t2 * bkstep, boffs);
        BAR; G_WAITB(B1); G_MMA(0, 1, At, B1); BAR;
        G_LDA(At, 0, 1); G_STAGE(G_SA(0, 0), gA0 + t2 * akstep, aoffs);
        BAR; WAIT_L(0); G_MMA(1, 0, At, B0); BAR; SCHED;
        G_STAGE(G_SB(0, 1), gB1 + t2 * bkstep, boffs);
        WAIT_V(6); BAR; G_MMA(1, 1, At, B1); BAR;
        G_LDB(B0, 2 * HTB, 0); SCHED; G_LDA(At, 1, 0); G_STAGE(G_SA(0, 1), gA1 + t2 * akstep, aoffs);
        WAIT_L(8); BAR; G_WAITB(B0); G_MMA(0, 0, At, B0); BAR; SCHED;
        G_LDB(B1, 2 * HTB, 1); G_STAGE(G_SB(1, 0), gB0 + t3 * bkstep, boffs);
        BAR; G_WAITB(B1); G_MMA(0, 1, At, B1); BAR;
        G_LDA(At, 1, 1); G_STAGE(G_SA(1, 0), gA0 + t3 * akstep, aoffs);
        BAR; WAIT_L(0); G_MMA(1, 0, At, B0); BAR; SCHED;
        G_STAGE(G_SB(1, 1), gB1 + t3 * bkstep, boffs);
        WAIT_V(6); BAR; G_MMA(1, 1, At, B1); BAR;
    }
    STAMP_AT();
    WAIT_V(0);
    if (wr == 0) BAR;
    unsigned pfv = 0;
    { const unsigned li = blockIdx.x * 512u + threadIdx.x; if (li < (unsigned)((size_t)F_ * D_ * 2 / 128)) pfv = pf[(size_t)li * 32]; }
    const int pcol = (fq & 1) ? 16 + 4 * (fq - 1) : 4 * fq;
#pragma unroll
    for (int ai = 0; ai < 2; ++ai)
#pragma unroll
        for (int m = 0; m < 4; ++m) {
            const int lrow = ai * 128 + wr * 64 + m * 16 + fr, row = m0 + lrow;
            u32x2 w[2];
#pragma unroll
            for (int n = 0; n < 2; ++n) {
                const f32x4 g = acc[ai][0][m][n], u = acc[ai][1][m][n];
                float o[4];
#pragma unroll
                for (int j = 0; j < 4; ++j) o[j] = g[j] * __builtin_amdgcn_rcpf(1.0f + __builtin_amdgcn_exp2f(g[j] * -1.4426950408889634f)) * u[j];
                w[n].x = pk_bf16(o[0], o[1]); w[n].y = pk_bf16(o[2], o[3]);
            }
            *(u32x4*)(act + (size_t)row * F_ + f0 + wc * 32 + pcol) = pair_swap(w[0], w[1]);
        }
    asm volatile("" :: "v"(pfv));
    WAIT_V(0); STAMP_AT(); STAMP_RT2();
#undef G_SA
#undef G_SB
#undef G_STAGE
#undef G_LDA
#undef G_LDB
#undef G_MMA
#undef G_WAITB
#undef G_FR
}

constexpr int AT_QB = 256, AT_ROWS = AT_QB + 256, AT_NW = AT_QB / 16;
__global__ __launch_bounds__(AT_NW * 64, 4) void attn_kernel(const bf16_t* __restrict__ qkv, bf16_t* __restrict__ o) {
    __shared__ __attribute__((aligned(16))) unsigned char sm[2 * AT_ROWS * 64];
    LAS unsigned char* lk = (LAS unsigned char*)sm; LAS unsigned char* lv = lk + AT_ROWS * 64;
    const int tid = threadIdx.x, wid = __builtin_amdgcn_readfirstlane(tid >> 6), lane = tid & 63, fr = lane & 15, fq = lane >> 4;
    const int head = blockIdx.x & 31, qb = blockIdx.x >> 5, q0 = qb * AT_QB;
    const bf16_t* Q = qkv + (size_t)head * S_ * 32;
    const bf16_t* Kg = qkv + (size_t)(32 + head) * S_ * 32;
    const bf16_t* Vg = qkv + (size_t)(64 + head) * S_ * 32;
#pragma unroll
    for (int pc = wid; pc < AT_ROWS / 16; pc += AT_NW) {
        const int row = pc * 16 + (lane >> 2), slot = lane & 3;
        int key = q0 - 256 + row; key = key < 0 ? 0 : (key > S_ - 1 ? S_ - 1 : key);
        const int chk = slot ^ ((-(row >> 2)) & 3), chv = slot ^ (((row >> 2) & 1) << 1);
        glds16(Kg + (size_t)key * 32 + chk * 8, lk + pc * 1024);
        glds16(Vg + (size_t)key * 32 + chv * 8, lv + pc * 1024);
    }
    const int qrow = q0 + wid * 16 + fr;
    bf16x8 qf;
    asm volatile("global_load_dwordx4 %0, %1, off" : "=v"(qf) : "v"(Q + (size_t)qrow * 32 + fq * 8));
    asm volatile("s_waitcnt vmcnt(0)" : "+v"(qf) :: "memory");
    __builtin_amdgcn_s_barrier();
    const int kbase = q0 + wid * 16 - 256;
    const int istart = kbase < 0 ? (-kbase) >> 4 : 0;
    f32x4 s[17];
    const int koff = (wid * 16 + fr) * 64 + ((fq ^ ((-(fr >> 2)) & 3)) * 16);
#pragma unroll
    for (int i = 0; i < 17; ++i) {
        const bf16x8 kf = *(const LAS bf16x8*)(lk + koff + i * 1024);
        s[i] = __builtin_amdgcn_mfma_f32_16x16x32_bf16(kf, qf, (f32x4){0.f, 0.f, 0.f, 0.f}, 0, 0, 0);
    }
    asm volatile("s_nop 7\n\ts_nop 7\n\ts_nop 3" : "+v"(s[0]), "+v"(s[1]), "+v"(s[2]), "+v"(s[3]), "+v"(s[4]), "+v"(s[5]), "+v"(s[6]), "+v"(s[7]), "+v"(s[8]),
                 "+v"(s[9]), "+v"(s[10]), "+v"(s[11]), "+v"(s[12]), "+v"(s[13]), "+v"(s[14]), "+v"(s[15]), "+v"(s[16]));
    const float NEG = -3.0e38f;
#pragma unroll
    for (int r = 0; r < 4; ++r) { if (!(4 * fq + r > fr)) s[0][r] = NEG; if (!(4 * fq + r <= fr)) s[16][r] = NEG; }
    if (istart > 0) {
#pragma unroll
        for (int i = 0; i < 16; ++i) if (i < istart) s[i] = (f32x4){NEG, NEG, NEG, NEG};
    }
    float mx = NEG;
#pragma unroll
    for (int i = 0; i < 17; ++i) { asm("v_max3_f32 %0, %1, %2, %3" : "=v"(mx) : "v"(mx), "v"(s[i][0]), "v"(s[i][1]));
                                   asm("v_max3_f32 %0, %1, %2, %3" : "=v"(mx) : "v"(mx), "v"(s[i][2]), "v"(s[i][3])); }
    mx = fmaxf(mx, __shfl_xor(mx, 16)); mx = fmaxf(mx, __shfl_xor(mx, 32));
    const float c = 0.17677669529663687f * 1.4426950408889634f;
    const float mc = -mx * c;
    typedef float f32x2 __attribute__((ext_vector_type(2)));
    const f32x2 c2 = {c, c}, mc2 = {mc, mc};
#pragma unroll
    for (int i = 0; i < 17; ++i) {
        const f32x2 lo = __builtin_elementwise_fma((f32x2){s[i][0], s[i][1]}, c2, mc2), hi = __builtin_elementwise_fma((f32x2){s[i][2], s[i][3]}, c2, mc2);
        s[i][0] = __builtin_amdgcn_exp2f(lo.x); s[i][1] = __builtin_amdgcn_exp2f(lo.y); s[i][2] = __builtin_amdgcn_exp2f(hi.x); s[i][3] = __builtin_amdgcn_exp2f(hi.y);
    }
    f32x4 oacc[2] = {(f32x4){0.f, 0.f, 0.f, 0.f}, (f32x4){0.f, 0.f, 0.f, 0.f}};
    f32x4 osum = (f32x4){0.f, 0.f, 0.f, 0.f};
    const bf16x8 ones = (bf16x8){0x3F80, 0x3F80, 0x3F80, 0x3F80, 0x3F80, 0x3F80, 0x3F80, 0x3F80};
    const int q4 = (lane & 15) >> 2, p4 = lane & 3;
    const unsigned va0 = (unsigned)(size_t)lv + (wid * 16 + 4 * fq + q4) * 64 + p4 * 8 + ((fq & 1) << 5), va1 = va0 ^ 32;
    s16x4 vr[2][4];
#define V_READ(SET, cc) do { vr[SET][0] = tr_read_a<(cc) * 2048>(va0); vr[SET][1] = tr_read_a<(cc) * 2048 + ((cc) < 8 ? 1024 : 0)>(va0); \
        vr[SET][2] = tr_read_a<(cc) * 2048>(va1); vr[SET][3] = tr_read_a<(cc) * 2048 + ((cc) < 8 ? 1024 : 0)>(va1); } while (0)
#define V_STEP(cc) do { constexpr int cur = (cc) & 1; \
        if constexpr ((cc) < 8) { V_READ(cur ^ 1, (cc) + 1); asm volatile("s_waitcnt lgkmcnt(4)" : "+v"(vr[cur][0]), "+v"(vr[cur][1]), "+v"(vr[cur][2]), "+v"(vr[cur][3])); } \
        else asm volatile("s_waitcnt lgkmcnt(0)" : "+v"(vr[cur][0]), "+v"(vr[cur][1]), "+v"(vr[cur][2]), "+v"(vr[cur][3])); \
        u32x4 pw; pw.x = pk_bf16(s[2 * (cc)][0], s[2 * (cc)][1]); pw.y = pk_bf16(s[2 * (cc)][2], s[2 * (cc)][3]); \
        if constexpr ((cc) < 8) { pw.z = pk_bf16(s[2 * (cc) + 1][0], s[2 * (cc) + 1][1]); pw.w = pk_bf16(s[2 * (cc) + 1][2], s[2 * (cc) + 1][3]); } else { pw.z = 0u; pw.w = 0u; } \
        const bf16x8 pf = __builtin_bit_cast(bf16x8, pw); \
        osum = __builtin_amdgcn_mfma_f32_16x16x32_bf16(ones, pf, osum, 0, 0, 0); \
        oacc[0] = __builtin_amdgcn_mfma_f32_16x16x32_bf16(__builtin_shufflevector(vr[cur][0], vr[cur][1], 0, 1, 2, 3, 4, 5, 6, 7), pf, oacc[0], 0, 0, 0); \
        oacc[1] = __builtin_amdgcn_mfma_f32_16x16x32_bf16(__builtin_shufflevector(vr[cur][2], vr[cur][3], 0, 1, 2, 3, 4, 5, 6, 7), pf, oacc[1], 0, 0, 0); } while (0)
    V_READ(0, 0);
    V_STEP(0); V_STEP(1); V_STEP(2); V_STEP(3); V_STEP(4); V_STEP(5); V_STEP(6); V_STEP(7); V_STEP(8);
#undef V_READ
#undef V_STEP
    const float inv = 1.0f / osum[0];
#pragma unroll
    for (int dt = 0; dt < 2; ++dt) {
        u32x2 w; w.x = pk_bf16(oacc[dt][0] * inv, oacc[dt][1] * inv); w.y = pk_bf16(oacc[dt][2] * inv, oacc[dt][3] * inv);
        *(u32x2*)(o + (size_t)qrow * D_ + head * 32 + dt * 16 + 4 * fq) = w;
    }
}

extern "C" void kernel_launch(void* const* d_in, const int* in_sizes, int n_in, void* d_out, int out_size, void* d_ws, size_t ws_size,
                              hipStream_t stream) {
    const bf16_t* x = (const bf16_t*)d_in[0];
    const bf16_t* wqkv = (const bf16_t*)d_in[1];
    const bf16_t* wo = (const bf16_t*)d_in[2];
    const bf16_t* w1 = (const bf16_t*)d_in[4];
    const bf16_t* w3 = (const bf16_t*)d_in[5];
    const bf16_t* w2 = (const bf16_t*)d_in[6];
    bf16_t* out = (bf16_t*)d_out;
    char* ws = (char*)d_ws;
    bf16_t* xn = (bf16_t*)(ws);
    bf16_t* qkv = (bf16_t*)(ws + (size_t)4 * 1024 * 1024);
    bf16_t* o = (bf16_t*)(ws + (size_t)20 * 1024 * 1024);
    bf16_t* h = (bf16_t*)(ws + (size_t)24 * 1024 * 1024);
    bf16_t* hn = (bf16_t*)(ws + (size_t)28 * 1024 * 1024);
    bf16_t* act = (bf16_t*)(ws + (size_t)32 * 1024 * 1024);

    {
        auto kern = gemm_pp<128, 192, 2, 4, 3, EPI_QKV, 8, 4, 1024>;
        const int shm = 3 * (128 + 192) * 128;
        (void)hipFuncSetAttribute((const void*)kern, hipFuncAttributeMaxDynamicSharedMemorySize, shm);
        kern<<<256, 512, shm, stream>>>(QKV_LNF ? x : xn, D_, wqkv, 3 * D_, wo, qkv, 16);
    }
    attn_kernel<<<32 * (S_ / AT_QB), AT_NW * 64, 0, stream>>>(qkv, o);
    {
        auto kern = gemm_pp<128, 64, 4, 2, 6, EPI_RES, 4, 8, 1024>;
        const int shm = 6 * (128 + 64) * 128;
        (void)hipFuncSetAttribute((const void*)kern, hipFuncAttributeMaxDynamicSharedMemorySize, shm);
        kern<<<256, 512, shm, stream>>>(o, D_, wo, D_, x, h, 16);
    }
    ln_kernel<<<S_ / 4, 256, 0, stream>>>(h, hn, (const unsigned*)w2, 0u, (const unsigned*)w2, 0u);
    {
        const int shm = 8 * 16384;
        (void)hipFuncSetAttribute((const void*)gemm_gateup, hipFuncAttributeMaxDynamicSharedMemorySize, shm);
        gemm_gateup<<<224, 512, shm, stream>>>(hn, w1, w3, act, (const unsigned*)w2);
    }
    {
        auto kern = gemm_pp<128, 64, 4, 2, 6, EPI_RES, 4, 8, 3584>;
        const int shm = 6 * (128 + 64) * 128;
        (void)hipFuncSetAttribute((const void*)kern, hipFuncAttributeMaxDynamicSharedMemorySize, shm);
        kern<<<256, 512, shm, stream>>>(act, F_, w2, D_, h, out, 16);
    }
}
```

```cpp
#include <hip/hip_runtime.h>
#include <hip/hip_bf16.h>
#include <stdint.h>

typedef unsigned short bf16_t;
typedef short bf16x8 __attribute__((ext_vector_type(8)));
typedef short s16x4 __attribute__((ext_vector_type(4)));
typedef float f32x4 __attribute__((ext_vector_type(4)));
typedef unsigned u32x4 __attribute__((ext_vector_type(4)));
typedef unsigned u32x2 __attribute__((ext_vector_type(2)));
#define LAS __attribute__((address_space(3)))
#define DI __device__ __forceinline__

constexpr int S_ = 2048, D_ = 1024, F_ = 3584, H_ = 32, DH_ = 32, WIN_ = 256;

DI float bf2f(unsigned short b) { return __uint_as_float(((unsigned)b) << 16); }
DI unsigned pk_bf16(float lo, float hi) {
    typedef __bf16 bf2 __attribute__((ext_vector_type(2)));
    bf2 v; v.x = (__bf16)lo; v.y = (__bf16)hi;
    return __builtin_bit_cast(unsigned, v);
}
DI s16x4 tr_read(LAS unsigned char* p) { return __builtin_amdgcn_ds_read_tr16_b64_v4i16((LAS s16x4*)p); }
template <int OFF> DI s16x4 tr_read_a(unsigned addr) { s16x4 r; asm volatile("ds_read_b64_tr_b16 %0, %1 offset:%2" : "=v"(r) : "v"(addr), "i"(OFF)); return r; }
DI void glds16(const void* g, LAS unsigned char* l) {
    __builtin_amdgcn_global_load_lds((const __attribute__((address_space(1))) unsigned*)g, (LAS unsigned*)l, 16, 0, 0);
}
#define WAIT_V(n) asm volatile("s_waitcnt vmcnt(" #n ")" ::: "memory")
#define WAIT_L(n) asm volatile("s_waitcnt lgkmcnt(" #n ")" ::: "memory")
#define BAR __builtin_amdgcn_s_barrier()
#define SCHED __builtin_amdgcn_sched_barrier(0)
#define STAMP_DECL(kid)
#define STAMP_AT()
#define STAMP_RT()
#define STAMP_RT2()

__global__ __launch_bounds__(256) void ln_kernel(const bf16_t* __restrict__ in, bf16_t* __restrict__ out, const unsigned* __restrict__ pf, unsigned pf_lines,
                                                 const unsigned* __restrict__ pfb, unsigned pfb_lines) {
    const int row = blockIdx.x * 4 + (threadIdx.x >> 6), lane = threadIdx.x & 63;
    const u32x4* p = (const u32x4*)(in + (size_t)row * D_);
    u32x4 a = p[lane], b = p[lane + 64];
    unsigned pfv = 0;
    { const unsigned li = blockIdx.x * 256u + threadIdx.x; if (li < pf_lines) pfv = pf[(size_t)li * 32];
      else if (li - pf_lines < pfb_lines) pfv = pfb[(size_t)(li - pf_lines) * 32]; }
    float v[16];
#pragma unroll
    for (int i = 0; i < 4; ++i) { v[2 * i] = __uint_as_float(a[i] << 16); v[2 * i + 1] = __uint_as_float(a[i] & 0xffff0000u);
                                  v[8 + 2 * i] = __uint_as_float(b[i] << 16); v[8 + 2 * i + 1] = __uint_as_float(b[i] & 0xffff0000u); }
    float s = 0.f;
#pragma unroll
    for (int i = 0; i < 16; ++i) s += v[i];
#pragma unroll
    for (int o = 32; o >= 1; o >>= 1) s += __shfl_xor(s, o);
    const float mean = s * (1.0f / D_);
    float q = 0.f;
#pragma unroll
    for (int i = 0; i < 16; ++i) { v[i] -= mean; q += v[i] * v[i]; }
#pragma unroll
    for (int o = 32; o >= 1; o >>= 1) q += __shfl_xor(q, o);
    const float rstd = 1.0f / sqrtf(q * (1.0f / D_) + 1e-5f);
    u32x4 oa, ob;
#pragma unroll
    for (int i = 0; i < 4; ++i) { oa[i] = pk_bf16(v[2 * i] * rstd, v[2 * i + 1] * rstd); ob[i] = pk_bf16(v[8 + 2 * i] * rstd, v[8 + 2 * i + 1] * rstd); }
    u32x4* po = (u32x4*)(out + (size_t)row * D_);
    po[lane] = oa; po[lane + 64] = ob;
    asm volatile("" :: "v"(pfv));
}

DI int bx_of_k(int k) { return ((k >> 1) & 1) | (((k >> 3) & 1) << 1); }

enum { EPI_QKV = 0, EPI_RES = 1 };
#define QKV_LNF 1
DI u32x4 pair_swap(u32x2 t0, u32x2 t1) {
    auto a = __builtin_amdgcn_permlane16_swap(t0.x, t1.x, false, false);
    auto b = __builtin_amdgcn_permlane16_swap(t0.y, t1.y, false, false);
    return (u32x4){a[0], b[0], a[1], b[1]};
}

template <int N> DI void wait_v() { asm volatile("s_waitcnt vmcnt(%0)" :: "i"(N) : "memory"); }
template <int OFF> DI bf16x8 ld128_a(unsigned addr) { bf16x8 r; asm volatile("ds_read_b128 %0, %1 offset:%2" : "=v"(r) : "v"(addr), "i"(OFF)); return r; }
typedef __bf16 bf2_t __attribute__((ext_vector_type(2)));
DI float dot2_one(unsigned w, float c) { return __builtin_amdgcn_fdot2_f32_bf16(__builtin_bit_cast(bf2_t, w), __builtin_bit_cast(bf2_t, 0x3F803F80u), c, false); }
DI float dot2_sq(unsigned w, float c) { return __builtin_amdgcn_fdot2_f32_bf16(__builtin_bit_cast(bf2_t, w), __builtin_bit_cast(bf2_t, w), c, false); }
DI void row_stats_acc(const bf16x8& f, float& rs, float& rq) { const u32x4 w = __builtin_bit_cast(u32x4, f);
#pragma unroll
    for (int i = 0; i < 4; ++i) { rs = dot2_one(w[i], rs); rq = dot2_sq(w[i], rq); } }
DI void col_sum_acc(const s16x4& lo, const s16x4& hi, float& cs) { const u32x2 a = __builtin_bit_cast(u32x2, lo), b = __builtin_bit_cast(u32x2, hi);
    cs = dot2_one(a.x, cs); cs = dot2_one(a.y, cs); cs = dot2_one(b.x, cs); cs = dot2_one(b.y, cs); }
DI float red_fq(float v) { v += __shfl_xor(v, 16); v += __shfl_xor(v, 32); return v; }

template <int BM, int BN, int WM, int WN, int NBUF, int EPI, int RM, int RN, int K>
__global__ __launch_bounds__(512, 2) void gemm_pp(const bf16_t* __restrict__ A, int lda, const bf16_t* __restrict__ W, int ldb,
                                                   const bf16_t* __restrict__ res, bf16_t* __restrict__ out, int tiles_m) {
    extern __shared__ __attribute__((aligned(16))) unsigned char smem_raw[];
    LAS unsigned char* lds = (LAS unsigned char*)smem_raw;
    constexpr int MT = BM / WM / 16, NT = BN / WN / 16;
    constexpr int NPA = BM / 64, NPB = BN / 64, NP = NPA + NPB;
    constexpr int ABYTES = BM * 128, TILE_BYTES = (BM + BN) * 128;
    constexpr int nt = K / 64;
    constexpr bool LNF = (EPI == EPI_QKV) && QKV_LNF;
    static_assert(WM * WN == 8, "8 waves");
    static_assert(MT == 2 || MT == 4, "MT");
    static_assert(!LNF || (MT == 4 && WN == 4 && WM == 2), "LN fold geometry");
    STAMP_DECL(EPI == EPI_QKV ? 0 : (K == 1024 ? 1 : 2)); STAMP_RT(); STAMP_AT();
    const int tid = threadIdx.x, wid = __builtin_amdgcn_readfirstlane(tid >> 6), lane = tid & 63;
    const int wm = wid / WN, wn = wid % WN, fr = lane & 15, fq = lane >> 4;
    int tm, tn;
    { const int bid = blockIdx.x, xcd = bid & 7, idx = bid >> 3; const int regm = tiles_m / RM;
      const int rr = xcd % regm, rc = xcd / regm; tm = rr * RM + (idx % RM); tn = rc * RN + (idx / RM); }
    const int m0 = tm * BM, n0 = tn * BN;
    const bf16_t* asrc[NPA]; const bf16_t* bsrc[NPB];
#pragma unroll
    for (int i = 0; i < NPA; ++i) { const int row = (wid + 8 * i) * 8 + (lane >> 3), ch = (lane & 7) ^ ((row >> 1) & 7);
        asrc[i] = A + (size_t)(m0 + row) * lda + ch * 8; }
#pragma unroll
    for (int j = 0; j < NPB; ++j) { const int k = wid * 8 + (lane >> 3), s16 = lane & 7, n16 = (s16 >> 1) ^ bx_of_k(k);
        bsrc[j] = W + (size_t)k * ldb + n0 + j * 64 + n16 * 16 + (s16 & 1) * 8; }
    const size_t bstep = (size_t)64 * ldb;
    auto stage = [&](int t, int buf) {
        LAS unsigned char* base = lds + buf * TILE_BYTES + wid * 1024;
#pragma unroll
        for (int i = 0; i < NPA; ++i) glds16(asrc[i] + (size_t)t * 64, base + i * 8192);
#pragma unroll
        for (int j = 0; j < NPB; ++j) glds16(bsrc[j] + (size_t)t * bstep, base + ABYTES + j * 8192);
    };
    const int rotm = LNF ? wn : 0, krot = LNF ? wm : 0;
    const int sw = (fr >> 1) & 7;
    unsigned aadr[MT][2], badr[NT][2];
#pragma unroll
    for (int j = 0; j < MT; ++j)
#pragma unroll
        for (int sl = 0; sl < 2; ++sl) aadr[j][sl] = (unsigned)(size_t)lds + (wm * MT * 16 + ((j + rotm) & (MT - 1)) * 16 + fr) * 128 + (((((sl ^ krot) * 4) + fq) ^ sw) * 16);
    { const int q = (lane & 15) >> 2, p = lane & 3, xl = ((q >> 1) & 1) | ((fq & 1) << 1);
#pragma unroll
      for (int n = 0; n < NT; ++n) { const int ntg = wn * NT + n;
          const unsigned b0 = (unsigned)(size_t)lds + ABYTES + (ntg >> 2) * 8192 + (8 * fq + q) * 128 + (((ntg & 3) ^ xl) * 32) + p * 8;
          badr[n][0] = b0 + krot * 4096; badr[n][1] = b0 + (1 - krot) * 4096; } }
    f32x4 acc[MT][NT];
#pragma unroll
    for (int m = 0; m < MT; ++m)
#pragma unroll
        for (int n = 0; n < NT; ++n) acc[m][n] = (f32x4){0.f, 0.f, 0.f, 0.f};
    f32x4 rsacc = (f32x4){0.f, 0.f, 0.f, 0.f}, rqacc = rsacc, csacc[NT];
#pragma unroll
    for (int n = 0; n < NT; ++n) csacc[n] = rsacc;
    const bf16x8 ones = (bf16x8){0x3F80, 0x3F80, 0x3F80, 0x3F80, 0x3F80, 0x3F80, 0x3F80, 0x3F80};
    bf16x8 afA[MT][2], afB[MT][2]; s16x4 bA[NT][4], bB[NT][4];
#define P_READ(AF, BR, buf) do { const unsigned bo_ = (unsigned)((buf) * TILE_BYTES); \
        if constexpr (LNF) { \
            _Pragma("unroll") for (int n = 0; n < NT; ++n) { const unsigned b0_ = badr[n][0] + bo_, b1_ = badr[n][1] + bo_; \
                BR[n][0] = tr_read_a<0>(b0_); BR[n][1] = tr_read_a<512>(b0_); BR[n][2] = tr_read_a<0>(b1_); BR[n][3] = tr_read_a<512>(b1_); } \
            _Pragma("unroll") for (int j = 0; j < MT; ++j) { AF[j][0] = ld128_a<0>(aadr[j][0] + bo_); AF[j][1] = ld128_a<0>(aadr[j][1] + bo_); } \
        } else { \
            _Pragma("unroll") for (int n = 0; n < NT; ++n) { const unsigned ba = badr[n][0] + bo_; \
                BR[n][0] = tr_read_a<0>(ba); BR[n][1] = tr_read_a<512>(ba); BR[n][2] = tr_read_a<4096>(ba); BR[n][3] = tr_read_a<4608>(ba); } \
            const unsigned a0_ = aadr[0][0] + bo_, a1_ = aadr[0][1] + bo_; \
            AF[0][0] = ld128_a<0>(a0_); AF[0][1] = ld128_a<0>(a1_); AF[1][0] = ld128_a<2048>(a0_); AF[1][1] = ld128_a<2048>(a1_); \
            if constexpr (MT == 4) { AF[MT - 2][0] = ld128_a<4096>(a0_); AF[MT - 2][1] = ld128_a<4096>(a1_); AF[MT - 1][0] = ld128_a<6144>(a0_); AF[MT - 1][1] = ld128_a<6144>(a1_); } } } while (0)
#define P_WAITL(AF, BR) do { _Pragma("unroll") for (int n = 0; n < NT; ++n) \
        asm volatile("s_waitcnt lgkmcnt(0)" : "+v"(BR[n][0]), "+v"(BR[n][1]), "+v"(BR[n][2]), "+v"(BR[n][3]) :: "memory"); \
        _Pragma("unroll") for (int m = 0; m < MT; ++m) asm volatile("" : "+v"(AF[m][0]), "+v"(AF[m][1])); } while (0)
#define P_MMA(AF, BR) do { _Pragma("unroll") for (int kk = 0; kk < 2; ++kk) _Pragma("unroll") for (int m = 0; m < MT; ++m) _Pragma("unroll") for (int n = 0; n < NT; ++n) \
        acc[m][n] = __builtin_amdgcn_mfma_f32_16x16x32_bf16(__builtin_shufflevector(BR[n][2 * kk], BR[n][2 * kk + 1], 0, 1, 2, 3, 4, 5, 6, 7), AF[m][kk], acc[m][n], 0, 0, 0); \
        if constexpr (LNF) { _Pragma("unroll") for (int kk = 0; kk < 2; ++kk) { rsacc = __builtin_amdgcn_mfma_f32_16x16x32_bf16(ones, AF[0][kk], rsacc, 0, 0, 0); \
                rqacc = __builtin_amdgcn_mfma_f32_16x16x32_bf16(AF[0][kk], AF[0][kk], rqacc, 0, 0, 0); } \
            _Pragma("unroll") for (int n = 0; n < NT; ++n) csacc[n] = __builtin_amdgcn_mfma_f32_16x16x32_bf16(__builtin_shufflevector(BR[n][0], BR[n][1], 0, 1, 2, 3, 4, 5, 6, 7), ones, csacc[n], 0, 0, 0); } } while (0)
#define P_STEP(AFC, BRC, AFN, BRN, FULL, WN_, RESLD) do { \
        P_WAITL(AFC, BRC); \
        wait_v<WN_>(); \
        BAR; \
        const int nb_ = (buf + 1 == NBUF) ? 0 : buf + 1; \
        if (FULL || t + 1 < nt) P_READ(AFN, BRN, nb_); \
        if (FULL || t + NBUF < nt) stage(t + NBUF, buf); \
        if (RESLD) { _Pragma("unroll") for (int m = 0; m < MT; ++m) _Pragma("unroll") for (int n = 0; n < NT; ++n) \
            rres[m][n] = *(const u32x2*)(res + (size_t)(m0 + (wm * MT + m) * 16 + fr) * D_ + n0 + (wn * NT + n) * 16 + 4 * fq); } \
        P_MMA(AFC, BRC); \
        buf = nb_; ++t; } while (0)
    u32x2 rres[MT][NT];
    constexpr int NR = (EPI == EPI_RES) ? MT * NT : 0;
#pragma unroll
    for (int t = 0; t < NBUF; ++t) stage(t, t);
    wait_v<(NBUF - 1) * NP>();
    BAR;
    STAMP_AT();
    P_READ(afA, bA, 0);
    int buf = 0, t = 0;
    constexpr int nmain = ((nt - NBUF) / 2) * 2;
    for (; t < nmain;) {
        P_STEP(afA, bA, afB, bB, true, (NBUF - 2) * NP, false);
        P_STEP(afB, bB, afA, bA, true, (NBUF - 2) * NP, false);
    }
#define P_TAILW(tt) ((((tt) + NBUF - 1 < nt - 1 ? (tt) + NBUF - 1 : nt - 1) - ((tt) + 1) > 0 ? (((tt) + NBUF - 1 < nt - 1 ? (tt) + NBUF - 1 : nt - 1) - ((tt) + 1)) : 0) * NP + ((tt) > nt - NBUF ? NR : 0))
#define P_TAIL2(tt) do { t = (tt); P_STEP(afA, bA, afB, bB, false, P_TAILW(tt), (NR > 0 && (tt) == nt - NBUF)); \
        P_STEP(afB, bB, afA, bA, false, P_TAILW((tt) + 1), (NR > 0 && (tt) + 1 == nt - NBUF)); } while (0)
    if constexpr (nmain + 0 < nt) P_TAIL2(nmain + 0);
    if constexpr (nmain + 2 < nt) P_TAIL2(nmain + 2);
    if constexpr (nmain + 4 < nt) P_TAIL2(nmain + 4);
    if constexpr (nmain + 6 < nt) P_TAIL2(nmain + 6);
    static_assert(nmain + 8 >= nt, "tail");
#undef P_TAILW
#undef P_TAIL2
    STAMP_AT();
#undef P_READ
#undef P_WAITL
#undef P_MMA
#undef P_STEP
    if constexpr (EPI == EPI_RES) {
#pragma unroll
        for (int m = 0; m < MT; ++m)
#pragma unroll
            for (int n = 0; n < NT; ++n) asm volatile("" : "+v"(rres[m][n]));
    }
    typedef float f32x2 __attribute__((ext_vector_type(2)));
    LAS f32x2* stat = (LAS f32x2*)lds;
    LAS float* cpart = (LAS float*)(lds + BM * 8);
    if constexpr (LNF) {
        const float rq = (fr & 2) ? ((fr & 1) ? rqacc[3] : rqacc[2]) : ((fr & 1) ? rqacc[1] : rqacc[0]);
        const float mean = rsacc[0] * (1.0f / K), var = fmaxf(rq * (1.0f / K) - mean * mean, 0.f);
        if (fq == (fr >> 2)) stat[wm * 64 + wn * 16 + fr] = (f32x2){mean, 1.0f / sqrtf(var + 1e-5f)};
#pragma unroll
        for (int n = 0; n < NT; ++n) if (fr == 0) *(LAS f32x4*)(cpart + wm * BN + (wn * NT + n) * 16 + 4 * fq) = csacc[n];
        __syncthreads();
    }
    unsigned pfv = 0;
    if constexpr (EPI == EPI_QKV) { const unsigned li = blockIdx.x * 512u + threadIdx.x; if (li < (unsigned)((size_t)D_ * D_ * 2 / 128)) pfv = ((const unsigned*)res)[(size_t)li * 32]; }
    const int pcol = (fq & 1) ? 16 + 4 * (fq - 1) : 4 * fq;
    f32x4 csum[NT];
    if constexpr (LNF) {
#pragma unroll
        for (int n = 0; n < NT; ++n) csum[n] = *(const LAS f32x4*)(cpart + (wn * NT + n) * 16 + 4 * fq) + *(const LAS f32x4*)(cpart + BN + (wn * NT + n) * 16 + 4 * fq);
    }
#pragma unroll
    for (int m = 0; m < MT; ++m) {
        const int lrow = (wm * MT + ((m + rotm) & (MT - 1))) * 16 + fr, row = m0 + lrow;
        f32x2 st; if constexpr (LNF) st = stat[lrow];
        u32x2 w[NT];
#pragma unroll
        for (int n = 0; n < NT; ++n) {
            f32x4 v = acc[m][n];
            if constexpr (LNF) v = (v - csum[n] * st.x) * st.y;
            if constexpr (EPI == EPI_RES) { const u32x2 r = rres[m][n];
                v[0] += __uint_as_float(r.x << 16); v[1] += __uint_as_float(r.x & 0xffff0000u);
                v[2] += __uint_as_float(r.y << 16); v[3] += __uint_as_float(r.y & 0xffff0000u); }
            w[n].x = pk_bf16(v[0], v[1]); w[n].y = pk_bf16(v[2], v[3]);
        }
#pragma unroll
        for (int n = 0; n + 1 < NT; n += 2) {
            const u32x4 o = pair_swap(w[n], w[n + 1]);
            const int col = n0 + (wn * NT + n) * 16 + pcol;
            if constexpr (EPI == EPI_QKV) *(u32x4*)(out + ((size_t)(col >> 5) * S_ + row) * 32 + (col & 31)) = o;
            else *(u32x4*)(out + (size_t)row * D_ + col) = o;
        }
        if constexpr (NT & 1) {
            const int col = n0 + (wn * NT + NT - 1) * 16 + 4 * fq;
            if constexpr (EPI == EPI_QKV) *(u32x2*)(out + ((size_t)(col >> 5) * S_ + row) * 32 + (col & 31)) = w[NT - 1];
            else *(u32x2*)(out + (size_t)row * D_ + col) = w[NT - 1];
        }
    }
    asm volatile("" :: "v"(pfv));
    WAIT_V(0); STAMP_AT(); STAMP_RT2();
}

__global__ __launch_bounds__(512, 2) void gemm_gateup(const bf16_t* __restrict__ A, const bf16_t* __restrict__ W1, const bf16_t* __restrict__ W3,
                                                       bf16_t* __restrict__ act, const unsigned* __restrict__ pf) {
    extern __shared__ __attribute__((aligned(16))) unsigned char smem_raw[];
    LAS unsigned char* lds = (LAS unsigned char*)smem_raw;
    constexpr int K = D_, HTB = 16384;
    STAMP_DECL(3); STAMP_RT(); STAMP_AT();
    const int tid = threadIdx.x, wid = __builtin_amdgcn_readfirstlane(tid >> 6), lane = tid & 63;
    const int wr = wid >> 2, wc = wid & 3, fr = lane & 15, fq = lane >> 4;
    if (blockIdx.x >= 224) {
        unsigned a = 0;
        for (unsigned li = (blockIdx.x - 224) * 512u + threadIdx.x; li < (unsigned)((size_t)F_ * D_ * 2 / 128); li += 32u * 512u) a += pf[(size_t)li * 32];
        asm volatile("" :: "v"(a));
        return;
    }
    int tm, tn;
    { const int bid = blockIdx.x, xcd = bid & 7, idx = bid >> 3; tm = (xcd & 1) * 4 + (idx & 3); tn = (xcd >> 1) * 7 + (idx >> 2); }
    const int m0 = tm * 256, f0 = tn * 128;
    constexpr int nt = K / 64;
    unsigned aoffs[2], boffs[2];
#pragma unroll
    for (int i = 0; i < 2; ++i) { const int row = (wid + 8 * i) * 8 + (lane >> 3), ch = (lane & 7) ^ ((row >> 1) & 7);
        aoffs[i] = (unsigned)(row * K + ch * 8) * 2u;
        const int k = wid * 8 + (lane >> 3), s16 = lane & 7, n16 = (s16 >> 1) ^ bx_of_k(k);
        boffs[i] = (unsigned)(k * F_ + i * 64 + n16 * 16 + (s16 & 1) * 8) * 2u; }
    const char* gA0 = (const char*)(A + (size_t)m0 * K); const char* gA1 = gA0 + (size_t)128 * K * 2;
    const char* gB0 = (const char*)(W1 + f0); const char* gB1 = (const char*)(W3 + f0);
    constexpr size_t akstep = 128, bkstep = (size_t)64 * F_ * 2;
    const unsigned ldsw = (unsigned)wid * 1024u;
#define G_SA(b, h) (((b) * 2 + (h)) * HTB)
#define G_SB(b, h) ((4 + (b) * 2 + (h)) * HTB)
#define G_STAGE(bufoff, gbase, voff) do { _Pragma("unroll") for (int _i = 0; _i < 2; ++_i) glds16((gbase) + (voff)[_i], lds + (bufoff) + ldsw + _i * 8192); } while (0)
    const int sw = (fr >> 1) & 7;
    const int aoff0 = (wr * 64 + fr) * 128 + (((wr * 4 + fq) ^ sw) * 16), aoff1 = aoff0 ^ 64;
    unsigned badr[2][2];
    { const int q = (lane & 15) >> 2, p = lane & 3, xl = ((q >> 1) & 1) | ((fq & 1) << 1);
#pragma unroll
      for (int n = 0; n < 2; ++n) { const int ntg = wc * 2 + n;
          const unsigned b0 = (unsigned)(size_t)lds + 4 * HTB + (ntg >> 2) * 8192 + (8 * fq + q) * 128 + (((ntg & 3) ^ xl) * 32) + p * 8;
          badr[n][0] = b0 + wr * 4096; badr[n][1] = b0 + (1 - wr) * 4096; } }
#define G_LDA(dst, b, h) do { _Pragma("unroll") for (int m = 0; m < 4; ++m) { dst[m][0] = *(const LAS bf16x8*)(lds + G_SA(b, h) + aoff0 + m * 2048); \
        dst[m][1] = *(const LAS bf16x8*)(lds + G_SA(b, h) + aoff1 + m * 2048); } } while (0)
#define G_LDB(dst, bo, h) do { _Pragma("unroll") for (int n = 0; n < 2; ++n) { const unsigned b0_ = badr[n][0] + (bo), b1_ = badr[n][1] + (bo); \
        dst[n][0] = tr_read_a<(h) * HTB>(b0_); dst[n][1] = tr_read_a<(h) * HTB + 512>(b0_); \
        dst[n][2] = tr_read_a<(h) * HTB>(b1_); dst[n][3] = tr_read_a<(h) * HTB + 512>(b1_); } } while (0)
#define G_WAITB(R) do { asm volatile("s_waitcnt lgkmcnt(0)" : "+v"(R[0][0]), "+v"(R[0][1]), "+v"(R[0][2]), "+v"(R[0][3]), "+v"(R[1][0]), "+v"(R[1][1]), "+v"(R[1][2]), "+v"(R[1][3]) :: "memory"); } while (0)
#define G_FR(R, n, k) __builtin_shufflevector(R[n][2 * (k)], R[n][2 * (k) + 1], 0, 1, 2, 3, 4, 5, 6, 7)
#define G_MMA(ai, bj, At, Bt) do { __builtin_amdgcn_s_setprio(1); _Pragma("unroll") for (int m = 0; m < 4; ++m) _Pragma("unroll") for (int n = 0; n < 2; ++n) \
        _Pragma("unroll") for (int k = 0; k < 2; ++k) acc[ai][bj][m][n] = __builtin_amdgcn_mfma_f32_16x16x32_bf16(G_FR(Bt, n, k), At[m][k], acc[ai][bj][m][n], 0, 0, 0); \
        __builtin_amdgcn_s_setprio(0); } while (0)
    f32x4 acc[2][2][4][2];
#pragma unroll
    for (int a = 0; a < 2; ++a)
#pragma unroll
        for (int b = 0; b < 2; ++b)
#pragma unroll
            for (int m = 0; m < 4; ++m)
#pragma unroll
                for (int n = 0; n < 2; ++n) acc[a][b][m][n] = (f32x4){0.f, 0.f, 0.f, 0.f};
    bf16x8 At[4][2]; s16x4 B0[2][4], B1[2][4];
    G_STAGE(G_SB(0, 0), gB0, boffs); G_STAGE(G_SA(0, 0), gA0, aoffs); G_STAGE(G_SB(0, 1), gB1, boffs); G_STAGE(G_SA(0, 1), gA1, aoffs);
    if (wr == 1) BAR;
    WAIT_V(4); BAR;
    G_STAGE(G_SB(1, 0), gB0 + bkstep, boffs); G_STAGE(G_SA(1, 0), gA0 + akstep, aoffs); G_STAGE(G_SB(1, 1), gB1 + bkstep, boffs);
    WAIT_V(6); BAR;
    STAMP_AT();
    for (int t = 0; t < nt; t += 2) {
        const int t1 = t + 1, t2 = (t + 2 < nt) ? t + 2 : nt - 1, t3 = (t + 3 < nt) ? t + 3 : nt - 1;
        G_LDB(B0, 0, 0); SCHED; G_LDA(At, 0, 0); G_STAGE(G_SA(1, 1), gA1 + t1 * akstep, aoffs);
        WAIT_L(8); BAR; G_WAITB(B0); G_MMA(0, 0, At, B0); BAR; SCHED;
        G_LDB(B1, 0, 1); G_STAGE(G_SB(0, 0), gB0 + t2 * bkstep, boffs);
        BAR; G_WAITB(B1); G_MMA(0, 1, At, B1); BAR;
        G_LDA(At, 0, 1); G_STAGE(G_SA(0, 0), gA0 + t2 * akstep, aoffs);
        BAR; WAIT_L(0); G_MMA(1, 0, At, B0); BAR; SCHED;
        G_STAGE(G_SB(0, 1), gB1 + t2 * bkstep, boffs);
        WAIT_V(6); BAR; G_MMA(1, 1, At, B1); BAR;
        G_LDB(B0, 2 * HTB, 0); SCHED; G_LDA(At, 1, 0); G_STAGE(G_SA(0, 1), gA1 + t2 * akstep, aoffs);
        WAIT_L(8); BAR; G_WAITB(B0); G_MMA(0, 0, At, B0); BAR; SCHED;
        G_LDB(B1, 2 * HTB, 1); G_STAGE(G_SB(1, 0), gB0 + t3 * bkstep, boffs);
        BAR; G_WAITB(B1); G_MMA(0, 1, At, B1); BAR;
        G_LDA(At, 1, 1); G_STAGE(G_SA(1, 0), gA0 + t3 * akstep, aoffs);
        BAR; WAIT_L(0); G_MMA(1, 0, At, B0); BAR; SCHED;
        G_STAGE(G_SB(1, 1), gB1 + t3 * bkstep, boffs);
        WAIT_V(6); BAR; G_MMA(1, 1, At, B1); BAR;
    }
    STAMP_AT();
    WAIT_V(0);
    if (wr == 0) BAR;
    const int pcol = (fq & 1) ? 16 + 4 * (fq - 1) : 4 * fq;
#pragma unroll
    for (int ai = 0; ai < 2; ++ai)
#pragma unroll
        for (int m = 0; m < 4; ++m) {
            const int lrow = ai * 128 + wr * 64 + m * 16 + fr, row = m0 + lrow;
            u32x2 w[2];
#pragma unroll
            for (int n = 0; n < 2; ++n) {
                const f32x4 g = acc[ai][0][m][n], u = acc[ai][1][m][n];
                float o[4];
#pragma unroll
                for (int j = 0; j < 4; ++j) o[j] = g[j] * __builtin_amdgcn_rcpf(1.0f + __builtin_amdgcn_exp2f(g[j] * -1.4426950408889634f)) * u[j];
                w[n].x = pk_bf16(o[0], o[1]); w[n].y = pk_bf16(o[2], o[3]);
            }
            *(u32x4*)(act + (size_t)row * F_ + f0 + wc * 32 + pcol) = pair_swap(w[0], w[1]);
        }
    WAIT_V(0); STAMP_AT(); STAMP_RT2();
#undef G_SA
#undef G_SB
#undef G_STAGE
#undef G_LDA
#undef G_LDB
#undef G_MMA
#undef G_WAITB
#undef G_FR
}

constexpr int AT_QB = 256, AT_ROWS = AT_QB + 256, AT_NW = AT_QB / 16;
__global__ __launch_bounds__(AT_NW * 64, 4) void attn_kernel(const bf16_t* __restrict__ qkv, bf16_t* __restrict__ o) {
    __shared__ __attribute__((aligned(16))) unsigned char sm[2 * AT_ROWS * 64];
    LAS unsigned char* lk = (LAS unsigned char*)sm; LAS unsigned char* lv = lk + AT_ROWS * 64;
    const int tid = threadIdx.x, wid = __builtin_amdgcn_readfirstlane(tid >> 6), lane = tid & 63, fr = lane & 15, fq = lane >> 4;
    const int head = blockIdx.x & 31, qb = blockIdx.x >> 5, q0 = qb * AT_QB;
    const bf16_t* Q = qkv + (size_t)head * S_ * 32;
    const bf16_t* Kg = qkv + (size_t)(32 + head) * S_ * 32;
    const bf16_t* Vg = qkv + (size_t)(64 + head) * S_ * 32;
#pragma unroll
    for (int pc = wid; pc < AT_ROWS / 16; pc += AT_NW) {
        const int row = pc * 16 + (lane >> 2), slot = lane & 3;
        int key = q0 - 256 + row; key = key < 0 ? 0 : (key > S_ - 1 ? S_ - 1 : key);
        const int chk = slot ^ ((-(row >> 2)) & 3), chv = slot ^ (((row >> 2) & 1) << 1);
        glds16(Kg + (size_t)key * 32 + chk * 8, lk + pc * 1024);
        glds16(Vg + (size_t)key * 32 + chv * 8, lv + pc * 1024);
    }
    const int qrow = q0 + wid * 16 + fr;
    bf16x8 qf;
    asm volatile("global_load_dwordx4 %0, %1, off" : "=v"(qf) : "v"(Q + (size_t)qrow * 32 + fq * 8));
    asm volatile("s_waitcnt vmcnt(0)" : "+v"(qf) :: "memory");
    __builtin_amdgcn_s_barrier();
    const int kbase = q0 + wid * 16 - 256;
    const int istart = kbase < 0 ? (-kbase) >> 4 : 0;
    f32x4 s[17];
    const int koff = (wid * 16 + fr) * 64 + ((fq ^ ((-(fr >> 2)) & 3)) * 16);
#pragma unroll
    for (int i = 0; i < 17; ++i) {
        const bf16x8 kf = *(const LAS bf16x8*)(lk + koff + i * 1024);
        s[i] = __builtin_amdgcn_mfma_f32_16x16x32_bf16(kf, qf, (f32x4){0.f, 0.f, 0.f, 0.f}, 0, 0, 0);
    }
    asm volatile("s_nop 7\n\ts_nop 7\n\ts_nop 3" : "+v"(s[0]), "+v"(s[1]), "+v"(s[2]), "+v"(s[3]), "+v"(s[4]), "+v"(s[5]), "+v"(s[6]), "+v"(s[7]), "+v"(s[8]),
                 "+v"(s[9]), "+v"(s[10]), "+v"(s[11]), "+v"(s[12]), "+v"(s[13]), "+v"(s[14]), "+v"(s[15]), "+v"(s[16]));
    const float NEG = -3.0e38f;
#pragma unroll
    for (int r = 0; r < 4; ++r) { if (!(4 * fq + r > fr)) s[0][r] = NEG; if (!(4 * fq + r <= fr)) s[16][r] = NEG; }
    if (istart > 0) {
#pragma unroll
        for (int i = 0; i < 16; ++i) if (i < istart) s[i] = (f32x4){NEG, NEG, NEG, NEG};
    }
    float mx = NEG;
#pragma unroll
    for (int i = 0; i < 17; ++i) { asm("v_max3_f32 %0, %1, %2, %3" : "=v"(mx) : "v"(mx), "v"(s[i][0]), "v"(s[i][1]));
                                   asm("v_max3_f32 %0, %1, %2, %3" : "=v"(mx) : "v"(mx), "v"(s[i][2]), "v"(s[i][3])); }
    mx = fmaxf(mx, __shfl_xor(mx, 16)); mx = fmaxf(mx, __shfl_xor(mx, 32));
    const float c = 0.17677669529663687f * 1.4426950408889634f;
    const float mc = -mx * c;
    typedef float f32x2 __attribute__((ext_vector_type(2)));
    const f32x2 c2 = {c, c}, mc2 = {mc, mc};
#pragma unroll
    for (int i = 0; i < 17; ++i) {
        const f32x2 lo = __builtin_elementwise_fma((f32x2){s[i][0], s[i][1]}, c2, mc2), hi = __builtin_elementwise_fma((f32x2){s[i][2], s[i][3]}, c2, mc2);
        s[i][0] = __builtin_amdgcn_exp2f(lo.x); s[i][1] = __builtin_amdgcn_exp2f(lo.y); s[i][2] = __builtin_amdgcn_exp2f(hi.x); s[i][3] = __builtin_amdgcn_exp2f(hi.y);
    }
    f32x4 oacc[2] = {(f32x4){0.f, 0.f, 0.f, 0.f}, (f32x4){0.f, 0.f, 0.f, 0.f}};
    f32x4 osum = (f32x4){0.f, 0.f, 0.f, 0.f};
    const bf16x8 ones = (bf16x8){0x3F80, 0x3F80, 0x3F80, 0x3F80, 0x3F80, 0x3F80, 0x3F80, 0x3F80};
    const int q4 = (lane & 15) >> 2, p4 = lane & 3;
    const unsigned va0 = (unsigned)(size_t)lv + (wid * 16 + 4 * fq + q4) * 64 + p4 * 8 + ((fq & 1) << 5), va1 = va0 ^ 32;
    s16x4 vr[2][4];
#define V_READ(SET, cc) do { vr[SET][0] = tr_read_a<(cc) * 2048>(va0); vr[SET][1] = tr_read_a<(cc) * 2048 + ((cc) < 8 ? 1024 : 0)>(va0); \
        vr[SET][2] = tr_read_a<(cc) * 2048>(va1); vr[SET][3] = tr_read_a<(cc) * 2048 + ((cc) < 8 ? 1024 : 0)>(va1); } while (0)
#define V_STEP(cc) do { constexpr int cur = (cc) & 1; \
        if constexpr ((cc) < 8) { V_READ(cur ^ 1, (cc) + 1); asm volatile("s_waitcnt lgkmcnt(4)" : "+v"(vr[cur][0]), "+v"(vr[cur][1]), "+v"(vr[cur][2]), "+v"(vr[cur][3])); } \
        else asm volatile("s_waitcnt lgkmcnt(0)" : "+v"(vr[cur][0]), "+v"(vr[cur][1]), "+v"(vr[cur][2]), "+v"(vr[cur][3])); \
        u32x4 pw; pw.x = pk_bf16(s[2 * (cc)][0], s[2 * (cc)][1]); pw.y = pk_bf16(s[2 * (cc)][2], s[2 * (cc)][3]); \
        if constexpr ((cc) < 8) { pw.z = pk_bf16(s[2 * (cc) + 1][0], s[2 * (cc) + 1][1]); pw.w = pk_bf16(s[2 * (cc) + 1][2], s[2 * (cc) + 1][3]); } else { pw.z = 0u; pw.w = 0u; } \
        const bf16x8 pf = __builtin_bit_cast(bf16x8, pw); \
        osum = __builtin_amdgcn_mfma_f32_16x16x32_bf16(ones, pf, osum, 0, 0, 0); \
        oacc[0] = __builtin_amdgcn_mfma_f32_16x16x32_bf16(__builtin_shufflevector(vr[cur][0], vr[cur][1], 0, 1, 2, 3, 4, 5, 6, 7), pf, oacc[0], 0, 0, 0); \
        oacc[1] = __builtin_amdgcn_mfma_f32_16x16x32_bf16(__builtin_shufflevector(vr[cur][2], vr[cur][3], 0, 1, 2, 3, 4, 5, 6, 7), pf, oacc[1], 0, 0, 0); } while (0)
    V_READ(0, 0);
    V_STEP(0); V_STEP(1); V_STEP(2); V_STEP(3); V_STEP(4); V_STEP(5); V_STEP(6); V_STEP(7); V_STEP(8);
#undef V_READ
#undef V_STEP
    const float inv = 1.0f / osum[0];
#pragma unroll
    for (int dt = 0; dt < 2; ++dt) {
        u32x2 w; w.x = pk_bf16(oacc[dt][0] * inv, oacc[dt][1] * inv); w.y = pk_bf16(oacc[dt][2] * inv, oacc[dt][3] * inv);
        *(u32x2*)(o + (size_t)qrow * D_ + head * 32 + dt * 16 + 4 * fq) = w;
    }
}

extern "C" void kernel_launch(void* const* d_in, const int* in_sizes, int n_in, void* d_out, int out_size, void* d_ws, size_t ws_size,
                              hipStream_t stream) {
    const bf16_t* x = (const bf16_t*)d_in[0];
    const bf16_t* wqkv = (const bf16_t*)d_in[1];
    const bf16_t* wo = (const bf16_t*)d_in[2];
    const bf16_t* w1 = (const bf16_t*)d_in[4];
    const bf16_t* w3 = (const bf16_t*)d_in[5];
    const bf16_t* w2 = (const bf16_t*)d_in[6];
    bf16_t* out = (bf16_t*)d_out;
    char* ws = (char*)d_ws;
    bf16_t* xn = (bf16_t*)(ws);
    bf16_t* qkv = (bf16_t*)(ws + (size_t)4 * 1024 * 1024);
    bf16_t* o = (bf16_t*)(ws + (size_t)20 * 1024 * 1024);
    bf16_t* h = (bf16_t*)(ws + (size_t)24 * 1024 * 1024);
    bf16_t* hn = (bf16_t*)(ws + (size_t)28 * 1024 * 1024);
    bf16_t* act = (bf16_t*)(ws + (size_t)32 * 1024 * 1024);

    {
        auto kern = gemm_pp<128, 192, 2, 4, 3, EPI_QKV, 8, 4, 1024>;
        const int shm = 3 * (128 + 192) * 128;
        (void)hipFuncSetAttribute((const void*)kern, hipFuncAttributeMaxDynamicSharedMemorySize, shm);
        kern<<<256, 512, shm, stream>>>(QKV_LNF ? x : xn, D_, wqkv, 3 * D_, wo, qkv, 16);
    }
    attn_kernel<<<32 * (S_ / AT_QB), AT_NW * 64, 0, stream>>>(qkv, o);
    {
        auto kern = gemm_pp<128, 64, 4, 2, 6, EPI_RES, 4, 8, 1024>;
        const int shm = 6 * (128 + 64) * 128;
        (void)hipFuncSetAttribute((const void*)kern, hipFuncAttributeMaxDynamicSharedMemorySize, shm);
        kern<<<256, 512, shm, stream>>>(o, D_, wo, D_, x, h, 16);
    }
    ln_kernel<<<S_ / 4, 256, 0, stream>>>(h, hn, (const unsigned*)w2, 0u, (const unsigned*)w2, 0u);
    {
        const int shm = 8 * 16384;
        (void)hipFuncSetAttribute((const void*)gemm_gateup, hipFuncAttributeMaxDynamicSharedMemorySize, shm);
        gemm_gateup<<<256, 512, shm, stream>>>(hn, w1, w3, act, (const unsigned*)w2);
    }
    {
        auto kern = gemm_pp<128, 64, 4, 2, 6, EPI_RES, 4, 8, 3584>;
        const int shm = 6 * (128 + 64) * 128;
        (void)hipFuncSetAttribute((const void*)kern, hipFuncAttributeMaxDynamicSharedMemorySize, shm);
        kern<<<256, 512, shm, stream>>>(act, F_, w2, D_, h, out, 16);
    }
}
```

```cpp
#include <hip/hip_runtime.h>
#include <hip/hip_bf16.h>
#include <stdint.h>

typedef unsigned short bf16_t;
typedef short bf16x8 __attribute__((ext_vector_type(8)));
typedef short s16x4 __attribute__((ext_vector_type(4)));
typedef float f32x4 __attribute__((ext_vector_type(4)));
typedef unsigned u32x4 __attribute__((ext_vector_type(4)));
typedef unsigned u32x2 __attribute__((ext_vector_type(2)));
#define LAS __attribute__((address_space(3)))
#define DI __device__ __forceinline__

constexpr int S_ = 2048, D_ = 1024, F_ = 3584, H_ = 32, DH_ = 32, WIN_ = 256;

DI float bf2f(unsigned short b) { return __uint_as_float(((unsigned)b) << 16); }
DI unsigned pk_bf16(float lo, float hi) {
    typedef __bf16 bf2 __attribute__((ext_vector_type(2)));
    bf2 v; v.x = (__bf16)lo; v.y = (__bf16)hi;
    return __builtin_bit_cast(unsigned, v);
}
DI s16x4 tr_read(LAS unsigned char* p) { return __builtin_amdgcn_ds_read_tr16_b64_v4i16((LAS s16x4*)p); }
template <int OFF> DI s16x4 tr_read_a(unsigned addr) { s16x4 r; asm volatile("ds_read_b64_tr_b16 %0, %1 offset:%2" : "=v"(r) : "v"(addr), "i"(OFF)); return r; }
DI void glds16(const void* g, LAS unsigned char* l) {
    __builtin_amdgcn_global_load_lds((const __attribute__((address_space(1))) unsigned*)g, (LAS unsigned*)l, 16, 0, 0);
}
#define WAIT_V(n) asm volatile("s_waitcnt vmcnt(" #n ")" ::: "memory")
#define WAIT_L(n) asm volatile("s_waitcnt lgkmcnt(" #n ")" ::: "memory")
#define BAR __builtin_amdgcn_s_barrier()
#define SCHED __builtin_amdgcn_sched_barrier(0)
#define STAMP_DECL(kid)
#define STAMP_AT()
#define STAMP_RT()
#define STAMP_RT2()

__global__ __launch_bounds__(256) void ln_kernel(const bf16_t* __restrict__ in, bf16_t* __restrict__ out, const unsigned* __restrict__ pf, unsigned pf_lines,
                                                 const unsigned* __restrict__ pfb, unsigned pfb_lines) {
    const int row = blockIdx.x * 4 + (threadIdx.x >> 6), lane = threadIdx.x & 63;
    const u32x4* p = (const u32x4*)(in + (size_t)row * D_);
    u32x4 a = p[lane], b = p[lane + 64];
    unsigned pfv = 0;
    { const unsigned li = blockIdx.x * 256u + threadIdx.x; if (li < pf_lines) pfv = pf[(size_t)li * 32];
      else if (li - pf_lines < pfb_lines) pfv = pfb[(size_t)(li - pf_lines) * 32]; }
    float v[16];
#pragma unroll
    for (int i = 0; i < 4; ++i) { v[2 * i] = __uint_as_float(a[i] << 16); v[2 * i + 1] = __uint_as_float(a[i] & 0xffff0000u);
                                  v[8 + 2 * i] = __uint_as_float(b[i] << 16); v[8 + 2 * i + 1] = __uint_as_float(b[i] & 0xffff0000u); }
    float s = 0.f;
#pragma unroll
    for (int i = 0; i < 16; ++i) s += v[i];
#pragma unroll
    for (int o = 32; o >= 1; o >>= 1) s += __shfl_xor(s, o);
    const float mean = s * (1.0f / D_);
    float q = 0.f;
#pragma unroll
    for (int i = 0; i < 16; ++i) { v[i] -= mean; q += v[i] * v[i]; }
#pragma unroll
    for (int o = 32; o >= 1; o >>= 1) q += __shfl_xor(q, o);
    const float rstd = 1.0f / sqrtf(q * (1.0f / D_) + 1e-5f);
    u32x4 oa, ob;
#pragma unroll
    for (int i = 0; i < 4; ++i) { oa[i] = pk_bf16(v[2 * i] * rstd, v[2 * i + 1] * rstd); ob[i] = pk_bf16(v[8 + 2 * i] * rstd, v[8 + 2 * i + 1] * rstd); }
    u32x4* po = (u32x4*)(out + (size_t)row * D_);
    po[lane] = oa; po[lane + 64] = ob;
    asm volatile("" :: "v"(pfv));
}

DI int bx_of_k(int k) { return ((k >> 1) & 1) | (((k >> 3) & 1) << 1); }

enum { EPI_QKV = 0, EPI_RES = 1 };
#define QKV_LNF 1
DI u32x4 pair_swap(u32x2 t0, u32x2 t1) {
    auto a = __builtin_amdgcn_permlane16_swap(t0.x, t1.x, false, false);
    auto b = __builtin_amdgcn_permlane16_swap(t0.y, t1.y, false, false);
    return (u32x4){a[0], b[0], a[1], b[1]};
}

template <int N> DI void wait_v() { asm volatile("s_waitcnt vmcnt(%0)" :: "i"(N) : "memory"); }
template <int OFF> DI bf16x8 ld128_a(unsigned addr) { bf16x8 r; asm volatile("ds_read_b128 %0, %1 offset:%2" : "=v"(r) : "v"(addr), "i"(OFF)); return r; }
typedef __bf16 bf2_t __attribute__((ext_vector_type(2)));
DI float dot2_one(unsigned w, float c) { return __builtin_amdgcn_fdot2_f32_bf16(__builtin_bit_cast(bf2_t, w), __builtin_bit_cast(bf2_t, 0x3F803F80u), c, false); }
DI float dot2_sq(unsigned w, float c) { return __builtin_amdgcn_fdot2_f32_bf16(__builtin_bit_cast(bf2_t, w), __builtin_bit_cast(bf2_t, w), c, false); }
DI void row_stats_acc(const bf16x8& f, float& rs, float& rq) { const u32x4 w = __builtin_bit_cast(u32x4, f);
#pragma unroll
    for (int i = 0; i < 4; ++i) { rs = dot2_one(w[i], rs); rq = dot2_sq(w[i], rq); } }
DI void col_sum_acc(const s16x4& lo, const s16x4& hi, float& cs) { const u32x2 a = __builtin_bit_cast(u32x2, lo), b = __builtin_bit_cast(u32x2, hi);
    cs = dot2_one(a.x, cs); cs = dot2_one(a.y, cs); cs = dot2_one(b.x, cs); cs = dot2_one(b.y, cs); }
DI float red_fq(float v) { v += __shfl_xor(v, 16); v += __shfl_xor(v, 32); return v; }

template <int BM, int BN, int WM, int WN, int NBUF, int EPI, int RM, int RN, int K>
__global__ __launch_bounds__(512, 2) void gemm_pp(const bf16_t* __restrict__ A, int lda, const bf16_t* __restrict__ W, int ldb,
                                                   const bf16_t* __restrict__ res, bf16_t* __restrict__ out, int tiles_m) {
    extern __shared__ __attribute__((aligned(16))) unsigned char smem_raw[];
    LAS unsigned char* lds = (LAS unsigned char*)smem_raw;
    constexpr int MT = BM / WM / 16, NT = BN / WN / 16;
    constexpr int NPA = BM / 64, NPB = BN / 64, NP = NPA + NPB;
    constexpr int ABYTES = BM * 128, TILE_BYTES = (BM + BN) * 128;
    constexpr int nt = K / 64;
    constexpr bool LNF = (EPI == EPI_QKV) && QKV_LNF;
    static_assert(WM * WN == 8, "8 waves");
    static_assert(MT == 2 || MT == 4, "MT");
    static_assert(!LNF || (MT == 4 && WN == 4 && WM == 2), "LN fold geometry");
    STAMP_DECL(EPI == EPI_QKV ? 0 : (K == 1024 ? 1 : 2)); STAMP_RT(); STAMP_AT();
    const int tid = threadIdx.x, wid = __builtin_amdgcn_readfirstlane(tid >> 6), lane = tid & 63;
    const int wm = wid / WN, wn = wid % WN, fr = lane & 15, fq = lane >> 4;
    int tm, tn;
    { const int bid = blockIdx.x, xcd = bid & 7, idx = bid >> 3; const int regm = tiles_m / RM;
      const int rr = xcd % regm, rc = xcd / regm; tm = rr * RM + (idx % RM); tn = rc * RN + (idx / RM); }
    const int m0 = tm * BM, n0 = tn * BN;
    const bf16_t* asrc[NPA]; const bf16_t* bsrc[NPB];
#pragma unroll
    for (int i = 0; i < NPA; ++i) { const int row = (wid + 8 * i) * 8 + (lane >> 3), ch = (lane & 7) ^ ((row >> 1) & 7);
        asrc[i] = A + (size_t)(m0 + row) * lda + ch * 8; }
#pragma unroll
    for (int j = 0; j < NPB; ++j) { const int k = wid * 8 + (lane >> 3), s16 = lane & 7, n16 = (s16 >> 1) ^ bx_of_k(k);
        bsrc[j] = W + (size_t)k * ldb + n0 + j * 64 + n16 * 16 + (s16 & 1) * 8; }
    const size_t bstep = (size_t)64 * ldb;
    auto stage = [&](int t, int buf) {
        LAS unsigned char* base = lds + buf * TILE_BYTES + wid * 1024;
#pragma unroll
        for (int i = 0; i < NPA; ++i) glds16(asrc[i] + (size_t)t * 64, base + i * 8192);
#pragma unroll
        for (int j = 0; j < NPB; ++j) glds16(bsrc[j] + (size_t)t * bstep, base + ABYTES + j * 8192);
    };
    const int rotm = LNF ? wn : 0, krot = LNF ? wm : 0;
    const int sw = (fr >> 1) & 7;
    unsigned aadr[MT][2], badr[NT][2];
#pragma unroll
    for (int j = 0; j < MT; ++j)
#pragma unroll
        for (int sl = 0; sl < 2; ++sl) aadr[j][sl] = (unsigned)(size_t)lds + (wm * MT * 16 + ((j + rotm) & (MT - 1)) * 16 + fr) * 128 + (((((sl ^ krot) * 4) + fq) ^ sw) * 16);
    { const int q = (lane & 15) >> 2, p = lane & 3, xl = ((q >> 1) & 1) | ((fq & 1) << 1);
#pragma unroll
      for (int n = 0; n < NT; ++n) { const int ntg = wn * NT + n;
          const unsigned b0 = (unsigned)(size_t)lds + ABYTES + (ntg >> 2) * 8192 + (8 * fq + q) * 128 + (((ntg & 3) ^ xl) * 32) + p * 8;
          badr[n][0] = b0 + krot * 4096; badr[n][1] = b0 + (1 - krot) * 4096; } }
    f32x4 acc[MT][NT];
#pragma unroll
    for (int m = 0; m < MT; ++m)
#pragma unroll
        for (int n = 0; n < NT; ++n) acc[m][n] = (f32x4){0.f, 0.f, 0.f, 0.f};
    f32x4 rsacc = (f32x4){0.f, 0.f, 0.f, 0.f}, rqacc = rsacc, csacc[NT];
#pragma unroll
    for (int n = 0; n < NT; ++n) csacc[n] = rsacc;
    const bf16x8 ones = (bf16x8){0x3F80, 0x3F80, 0x3F80, 0x3F80, 0x3F80, 0x3F80, 0x3F80, 0x3F80};
    bf16x8 afA[MT][2], afB[MT][2]; s16x4 bA[NT][4], bB[NT][4];
#define P_READ(AF, BR, buf) do { const unsigned bo_ = (unsigned)((buf) * TILE_BYTES); \
        if constexpr (LNF) { \
            _Pragma("unroll") for (int n = 0; n < NT; ++n) { const unsigned b0_ = badr[n][0] + bo_, b1_ = badr[n][1] + bo_; \
                BR[n][0] = tr_read_a<0>(b0_); BR[n][1] = tr_read_a<512>(b0_); BR[n][2] = tr_read_a<0>(b1_); BR[n][3] = tr_read_a<512>(b1_); } \
            _Pragma("unroll") for (int j = 0; j < MT; ++j) { AF[j][0] = ld128_a<0>(aadr[j][0] + bo_); AF[j][1] = ld128_a<0>(aadr[j][1] + bo_); } \
        } else { \
            _Pragma("unroll") for (int n = 0; n < NT; ++n) { const unsigned ba = badr[n][0] + bo_; \
                BR[n][0] = tr_read_a<0>(ba); BR[n][1] = tr_read_a<512>(ba); BR[n][2] = tr_read_a<4096>(ba); BR[n][3] = tr_read_a<4608>(ba); } \
            const unsigned a0_ = aadr[0][0] + bo_, a1_ = aadr[0][1] + bo_; \
            AF[0][0] = ld128_a<0>(a0_); AF[0][1] = ld128_a<0>(a1_); AF[1][0] = ld128_a<2048>(a0_); AF[1][1] = ld128_a<2048>(a1_); \
            if constexpr (MT == 4) { AF[MT - 2][0] = ld128_a<4096>(a0_); AF[MT - 2][1] = ld128_a<4096>(a1_); AF[MT - 1][0] = ld128_a<6144>(a0_); AF[MT - 1][1] = ld128_a<6144>(a1_); } } } while (0)
#define P_WAITL(AF, BR) do { _Pragma("unroll") for (int n = 0; n < NT; ++n) \
        asm volatile("s_waitcnt lgkmcnt(0)" : "+v"(BR[n][0]), "+v"(BR[n][1]), "+v"(BR[n][2]), "+v"(BR[n][3]) :: "memory"); \
        _Pragma("unroll") for (int m = 0; m < MT; ++m) asm volatile("" : "+v"(AF[m][0]), "+v"(AF[m][1])); } while (0)
#define P_MMA(AF, BR) do { _Pragma("unroll") for (int kk = 0; kk < 2; ++kk) _Pragma("unroll") for (int m = 0; m < MT; ++m) _Pragma("unroll") for (int n = 0; n < NT; ++n) \
        acc[m][n] = __builtin_amdgcn_mfma_f32_16x16x32_bf16(__builtin_shufflevector(BR[n][2 * kk], BR[n][2 * kk + 1], 0, 1, 2, 3, 4, 5, 6, 7), AF[m][kk], acc[m][n], 0, 0, 0); \
        if constexpr (LNF) { _Pragma("unroll") for (int kk = 0; kk < 2; ++kk) { rsacc = __builtin_amdgcn_mfma_f32_16x16x32_bf16(ones, AF[0][kk], rsacc, 0, 0, 0); \
                rqacc = __builtin_amdgcn_mfma_f32_16x16x32_bf16(AF[0][kk], AF[0][kk], rqacc, 0, 0, 0); } \
            _Pragma("unroll") for (int n = 0; n < NT; ++n) csacc[n] = __builtin_amdgcn_mfma_f32_16x16x32_bf16(__builtin_shufflevector(BR[n][0], BR[n][1], 0, 1, 2, 3, 4, 5, 6, 7), ones, csacc[n], 0, 0, 0); } } while (0)
#define P_STEP(AFC, BRC, AFN, BRN, FULL, WN_, RESLD) do { \
        P_WAITL(AFC, BRC); \
        wait_v<WN_>(); \
        BAR; \
        const int nb_ = (buf + 1 == NBUF) ? 0 : buf + 1; \
        if (FULL || t + 1 < nt) P_READ(AFN, BRN, nb_); \
        if (FULL || t + NBUF < nt) stage(t + NBUF, buf); \
        if (RESLD) { _Pragma("unroll") for (int m = 0; m < MT; ++m) _Pragma("unroll") for (int n = 0; n < NT; ++n) \
            rres[m][n] = *(const u32x2*)(res + (size_t)(m0 + (wm * MT + m) * 16 + fr) * D_ + n0 + (wn * NT + n) * 16 + 4 * fq); } \
        P_MMA(AFC, BRC); \
        buf = nb_; ++t; } while (0)
    u32x2 rres[MT][NT];
    constexpr int NR = (EPI == EPI_RES) ? MT * NT : 0;
#pragma unroll
    for (int t = 0; t < NBUF; ++t) stage(t, t);
    wait_v<(NBUF - 1) * NP>();
    BAR;
    STAMP_AT();
    P_READ(afA, bA, 0);
    int buf = 0, t = 0;
    constexpr int nmain = ((nt - NBUF) / 2) * 2;
    for (; t < nmain;) {
        P_STEP(afA, bA, afB, bB, true, (NBUF - 2) * NP, false);
        P_STEP(afB, bB, afA, bA, true, (NBUF - 2) * NP, false);
    }
#define P_TAILW(tt) ((((tt) + NBUF - 1 < nt - 1 ? (tt) + NBUF - 1 : nt - 1) - ((tt) + 1) > 0 ? (((tt) + NBUF - 1 < nt - 1 ? (tt) + NBUF - 1 : nt - 1) - ((tt) + 1)) : 0) * NP + ((tt) > nt - NBUF ? NR : 0))
#define P_TAIL2(tt) do { t = (tt); P_STEP(afA, bA, afB, bB, false, P_TAILW(tt), (NR > 0 && (tt) == nt - NBUF)); \
        P_STEP(afB, bB, afA, bA, false, P_TAILW((tt) + 1), (NR > 0 && (tt) + 1 == nt - NBUF)); } while (0)
    if constexpr (nmain + 0 < nt) P_TAIL2(nmain + 0);
    if constexpr (nmain + 2 < nt) P_TAIL2(nmain + 2);
    if constexpr (nmain + 4 < nt) P_TAIL2(nmain + 4);
    if constexpr (nmain + 6 < nt) P_TAIL2(nmain + 6);
    static_assert(nmain + 8 >= nt, "tail");
#undef P_TAILW
#undef P_TAIL2
    STAMP_AT();
#undef P_READ
#undef P_WAITL
#undef P_MMA
#undef P_STEP
    if constexpr (EPI == EPI_RES) {
#pragma unroll
        for (int m = 0; m < MT; ++m)
#pragma unroll
            for (int n = 0; n < NT; ++n) asm volatile("" : "+v"(rres[m][n]));
    }
    typedef float f32x2 __attribute__((ext_vector_type(2)));
    LAS f32x2* stat = (LAS f32x2*)lds;
    LAS float* cpart = (LAS float*)(lds + BM * 8);
    if constexpr (LNF) {
        const float rq = (fr & 2) ? ((fr & 1) ? rqacc[3] : rqacc[2]) : ((fr & 1) ? rqacc[1] : rqacc[0]);
        const float mean = rsacc[0] * (1.0f / K), var = fmaxf(rq * (1.0f / K) - mean * mean, 0.f);
        if (fq == (fr >> 2)) stat[wm * 64 + wn * 16 + fr] = (f32x2){mean, 1.0f / sqrtf(var + 1e-5f)};
#pragma unroll
        for (int n = 0; n < NT; ++n) if (fr == 0) *(LAS f32x4*)(cpart + wm * BN + (wn * NT + n) * 16 + 4 * fq) = csacc[n];
        __syncthreads();
    }
    unsigned pfv = 0;
    if constexpr (EPI == EPI_QKV) { const unsigned li = blockIdx.x * 512u + threadIdx.x; if (li < (unsigned)((size_t)D_ * D_ * 2 / 128)) pfv = ((const unsigned*)res)[(size_t)li * 32]; }
    const int pcol = (fq & 1) ? 16 + 4 * (fq - 1) : 4 * fq;
    f32x4 csum[NT];
    if constexpr (LNF) {
#pragma unroll
        for (int n = 0; n < NT; ++n) csum[n] = *(const LAS f32x4*)(cpart + (wn * NT + n) * 16 + 4 * fq) + *(const LAS f32x4*)(cpart + BN + (wn * NT + n) * 16 + 4 * fq);
    }
#pragma unroll
    for (int m = 0; m < MT; ++m) {
        const int lrow = (wm * MT + ((m + rotm) & (MT - 1))) * 16 + fr, row = m0 + lrow;
        f32x2 st; if constexpr (LNF) st = stat[lrow];
        u32x2 w[NT];
#pragma unroll
        for (int n = 0; n < NT; ++n) {
            f32x4 v = acc[m][n];
            if constexpr (LNF) v = (v - csum[n] * st.x) * st.y;
            if constexpr (EPI == EPI_RES) { const u32x2 r = rres[m][n];
                v[0] += __uint_as_float(r.x << 16); v[1] += __uint_as_float(r.x & 0xffff0000u);
                v[2] += __uint_as_float(r.y << 16); v[3] += __uint_as_float(r.y & 0xffff0000u); }
            w[n].x = pk_bf16(v[0], v[1]); w[n].y = pk_bf16(v[2], v[3]);
        }
#pragma unroll
        for (int n = 0; n + 1 < NT; n += 2) {
            const u32x4 o = pair_swap(w[n], w[n + 1]);
            const int col = n0 + (wn * NT + n) * 16 + pcol;
            if constexpr (EPI == EPI_QKV) *(u32x4*)(out + ((size_t)(col >> 5) * S_ + row) * 32 + (col & 31)) = o;
            else *(u32x4*)(out + (size_t)row * D_ + col) = o;
        }
        if constexpr (NT & 1) {
            const int col = n0 + (wn * NT + NT - 1) * 16 + 4 * fq;
            if constexpr (EPI == EPI_QKV) *(u32x2*)(out + ((size_t)(col >> 5) * S_ + row) * 32 + (col & 31)) = w[NT - 1];
            else *(u32x2*)(out + (size_t)row * D_ + col) = w[NT - 1];
        }
    }
    asm volatile("" :: "v"(pfv));
    WAIT_V(0); STAMP_AT(); STAMP_RT2();
}

__global__ __launch_bounds__(512, 2) void gemm_gateup(const bf16_t* __restrict__ A, const bf16_t* __restrict__ W1, const bf16_t* __restrict__ W3,
                                                       bf16_t* __restrict__ act, const unsigned* __restrict__ pf) {
    extern __shared__ __attribute__((aligned(16))) unsigned char smem_raw[];
    LAS unsigned char* lds = (LAS unsigned char*)smem_raw;
    constexpr int K = D_, HTB = 16384;
    STAMP_DECL(3); STAMP_RT(); STAMP_AT();
    const int tid = threadIdx.x, wid = __builtin_amdgcn_readfirstlane(tid >> 6), lane = tid & 63;
    const int wr = wid >> 2, wc = wid & 3, fr = lane & 15, fq = lane >> 4;
    if (blockIdx.x >= 224) {
        constexpr unsigned NL = (unsigned)((size_t)F_ * D_ * 2 / 128);
        const unsigned l0 = (blockIdx.x - 224) * 512u + threadIdx.x;
        unsigned a = 0;
#pragma unroll
        for (int i = 0; i < 4; ++i) { const unsigned li = l0 + i * 16384u; if (li < NL) a += ((const unsigned*)W1)[(size_t)li * 32] + ((const unsigned*)W3)[(size_t)li * 32]; }
#pragma unroll
        for (int i = 0; i < 4; ++i) { const unsigned li = l0 + i * 16384u; if (li < NL) a += pf[(size_t)li * 32]; }
        asm volatile("" :: "v"(a));
        return;
    }
    int tm, tn;
    { const int bid = blockIdx.x, xcd = bid & 7, idx = bid >> 3; tm = (xcd & 1) * 4 + (idx & 3); tn = (xcd >> 1) * 7 + (idx >> 2); }
    const int m0 = tm * 256, f0 = tn * 128;
    constexpr int nt = K / 64;
    unsigned aoffs[2], boffs[2];
#pragma unroll
    for (int i = 0; i < 2; ++i) { const int row = (wid + 8 * i) * 8 + (lane >> 3), ch = (lane & 7) ^ ((row >> 1) & 7);
        aoffs[i] = (unsigned)(row * K + ch * 8) * 2u;
        const int k = wid * 8 + (lane >> 3), s16 = lane & 7, n16 = (s16 >> 1) ^ bx_of_k(k);
        boffs[i] = (unsigned)(k * F_ + i * 64 + n16 * 16 + (s16 & 1) * 8) * 2u; }
    const char* gA0 = (const char*)(A + (size_t)m0 * K); const char* gA1 = gA0 + (size_t)128 * K * 2;
    const char* gB0 = (const char*)(W1 + f0); const char* gB1 = (const char*)(W3 + f0);
    constexpr size_t akstep = 128, bkstep = (size_t)64 * F_ * 2;
    const unsigned ldsw = (unsigned)wid * 1024u;
#define G_SA(b, h) (((b) * 2 + (h)) * HTB)
#define G_SB(b, h) ((4 + (b) * 2 + (h)) * HTB)
#define G_STAGE(bufoff, gbase, voff) do { _Pragma("unroll") for (int _i = 0; _i < 2; ++_i) glds16((gbase) + (voff)[_i], lds + (bufoff) + ldsw + _i * 8192); } while (0)
    const int sw = (fr >> 1) & 7;
    const int aoff0 = (wr * 64 + fr) * 128 + (((wr * 4 + fq) ^ sw) * 16), aoff1 = aoff0 ^ 64;
    unsigned badr[2][2];
    { const int q = (lane & 15) >> 2, p = lane & 3, xl = ((q >> 1) & 1) | ((fq & 1) << 1);
#pragma unroll
      for (int n = 0; n < 2; ++n) { const int ntg = wc * 2 + n;
          const unsigned b0 = (unsigned)(size_t)lds + 4 * HTB + (ntg >> 2) * 8192 + (8 * fq + q) * 128 + (((ntg & 3) ^ xl) * 32) + p * 8;
          badr[n][0] = b0 + wr * 4096; badr[n][1] = b0 + (1 - wr) * 4096; } }
#define G_LDA(dst, b, h) do { _Pragma("unroll") for (int m = 0; m < 4; ++m) { dst[m][0] = *(const LAS bf16x8*)(lds + G_SA(b, h) + aoff0 + m * 2048); \
        dst[m][1] = *(const LAS bf16x8*)(lds + G_SA(b, h) + aoff1 + m * 2048); } } while (0)
#define G_LDB(dst, bo, h) do { _Pragma("unroll") for (int n = 0; n < 2; ++n) { const unsigned b0_ = badr[n][0] + (bo), b1_ = badr[n][1] + (bo); \
        dst[n][0] = tr_read_a<(h) * HTB>(b0_); dst[n][1] = tr_read_a<(h) * HTB + 512>(b0_); \
        dst[n][2] = tr_read_a<(h) * HTB>(b1_); dst[n][3] = tr_read_a<(h) * HTB + 512>(b1_); } } while (0)
#define G_WAITB(R) do { asm volatile("s_waitcnt lgkmcnt(0)" : "+v"(R[0][0]), "+v"(R[0][1]), "+v"(R[0][2]), "+v"(R[0][3]), "+v"(R[1][0]), "+v"(R[1][1]), "+v"(R[1][2]), "+v"(R[1][3]) :: "memory"); } while (0)
#define G_FR(R, n, k) __builtin_shufflevector(R[n][2 * (k)], R[n][2 * (k) + 1], 0, 1, 2, 3, 4, 5, 6, 7)
#define G_MMA(ai, bj, At, Bt) do { __builtin_amdgcn_s_setprio(1); _Pragma("unroll") for (int m = 0; m < 4; ++m) _Pragma("unroll") for (int n = 0; n < 2; ++n) \
        _Pragma("unroll") for (int k = 0; k < 2; ++k) acc[ai][bj][m][n] = __builtin_amdgcn_mfma_f32_16x16x32_bf16(G_FR(Bt, n, k), At[m][k], acc[ai][bj][m][n], 0, 0, 0); \
        __builtin_amdgcn_s_setprio(0); } while (0)
    f32x4 acc[2][2][4][2];
#pragma unroll
    for (int a = 0; a < 2; ++a)
#pragma unroll
        for (int b = 0; b < 2; ++b)
#pragma unroll
            for (int m = 0; m < 4; ++m)
#pragma unroll
                for (int n = 0; n < 2; ++n) acc[a][b][m][n] = (f32x4){0.f, 0.f, 0.f, 0.f};
    bf16x8 At[4][2]; s16x4 B0[2][4], B1[2][4];
    G_STAGE(G_SB(0, 0), gB0, boffs); G_STAGE(G_SA(0, 0), gA0, aoffs); G_STAGE(G_SB(0, 1), gB1, boffs); G_STAGE(G_SA(0, 1), gA1, aoffs);
    if (wr == 1) BAR;
    WAIT_V(4); BAR;
    G_STAGE(G_SB(1, 0), gB0 + bkstep, boffs); G_STAGE(G_SA(1, 0), gA0 + akstep, aoffs); G_STAGE(G_SB(1, 1), gB1 + bkstep, boffs);
    WAIT_V(6); BAR;
    STAMP_AT();
    for (int t = 0; t < nt; t += 2) {
        const int t1 = t + 1, t2 = (t + 2 < nt) ? t + 2 : nt - 1, t3 = (t + 3 < nt) ? t + 3 : nt - 1;
        G_LDB(B0, 0, 0); SCHED; G_LDA(At, 0, 0); G_STAGE(G_SA(1, 1), gA1 + t1 * akstep, aoffs);
        WAIT_L(8); BAR; G_WAITB(B0); G_MMA(0, 0, At, B0); BAR; SCHED;
        G_LDB(B1, 0, 1); G_STAGE(G_SB(0, 0), gB0 + t2 * bkstep, boffs);
        BAR; G_WAITB(B1); G_MMA(0, 1, At, B1); BAR;
        G_LDA(At, 0, 1); G_STAGE(G_SA(0, 0), gA0 + t2 * akstep, aoffs);
        BAR; WAIT_L(0); G_MMA(1, 0, At, B0); BAR; SCHED;
        G_STAGE(G_SB(0, 1), gB1 + t2 * bkstep, boffs);
        WAIT_V(6); BAR; G_MMA(1, 1, At, B1); BAR;
        G_LDB(B0, 2 * HTB, 0); SCHED; G_LDA(At, 1, 0); G_STAGE(G_SA(0, 1), gA1 + t2 * akstep, aoffs);
        WAIT_L(8); BAR; G_WAITB(B0); G_MMA(0, 0, At, B0); BAR; SCHED;
        G_LDB(B1, 2 * HTB, 1); G_STAGE(G_SB(1, 0), gB0 + t3 * bkstep, boffs);
        BAR; G_WAITB(B1); G_MMA(0, 1, At, B1); BAR;
        G_LDA(At, 1, 1); G_STAGE(G_SA(1, 0), gA0 + t3 * akstep, aoffs);
        BAR; WAIT_L(0); G_MMA(1, 0, At, B0); BAR; SCHED;
        G_STAGE(G_SB(1, 1), gB1 + t3 * bkstep, boffs);
        WAIT_V(6); BAR; G_MMA(1, 1, At, B1); BAR;
    }
    STAMP_AT();
    WAIT_V(0);
    if (wr == 0) BAR;
    const int pcol = (fq & 1) ? 16 + 4 * (fq - 1) : 4 * fq;
#pragma unroll
    for (int ai = 0; ai < 2; ++ai)
#pragma unroll
        for (int m = 0; m < 4; ++m) {
            const int lrow = ai * 128 + wr * 64 + m * 16 + fr, row = m0 + lrow;
            u32x2 w[2];
#pragma unroll
            for (int n = 0; n < 2; ++n) {
                const f32x4 g = acc[ai][0][m][n], u = acc[ai][1][m][n];
                float o[4];
#pragma unroll
                for (int j = 0; j < 4; ++j) o[j] = g[j] * __builtin_amdgcn_rcpf(1.0f + __builtin_amdgcn_exp2f(g[j] * -1.4426950408889634f)) * u[j];
                w[n].x = pk_bf16(o[0], o[1]); w[n].y = pk_bf16(o[2], o[3]);
            }
            *(u32x4*)(act + (size_t)row * F_ + f0 + wc * 32 + pcol) = pair_swap(w[0], w[1]);
        }
    WAIT_V(0); STAMP_AT(); STAMP_RT2();
#undef G_SA
#undef G_SB
#undef G_STAGE
#undef G_LDA
#undef G_LDB
#undef G_MMA
#undef G_WAITB
#undef G_FR
}

constexpr int AT_QB = 256, AT_ROWS = AT_QB + 256, AT_NW = AT_QB / 16;
__global__ __launch_bounds__(AT_NW * 64, 4) void attn_kernel(const bf16_t* __restrict__ qkv, bf16_t* __restrict__ o) {
    __shared__ __attribute__((aligned(16))) unsigned char sm[2 * AT_ROWS * 64];
    LAS unsigned char* lk = (LAS unsigned char*)sm; LAS unsigned char* lv = lk + AT_ROWS * 64;
    const int tid = threadIdx.x, wid = __builtin_amdgcn_readfirstlane(tid >> 6), lane = tid & 63, fr = lane & 15, fq = lane >> 4;
    const int head = blockIdx.x & 31, qb = blockIdx.x >> 5, q0 = qb * AT_QB;
    const bf16_t* Q = qkv + (size_t)head * S_ * 32;
    const bf16_t* Kg = qkv + (size_t)(32 + head) * S_ * 32;
    const bf16_t* Vg = qkv + (size_t)(64 + head) * S_ * 32;
#pragma unroll
    for (int pc = wid; pc < AT_ROWS / 16; pc += AT_NW) {
        const int row = pc * 16 + (lane >> 2), slot = lane & 3;
        int key = q0 - 256 + row; key = key < 0 ? 0 : (key > S_ - 1 ? S_ - 1 : key);
        const int chk = slot ^ ((-(row >> 2)) & 3), chv = slot ^ (((row >> 2) & 1) << 1);
        glds16(Kg + (size_t)key * 32 + chk * 8, lk + pc * 1024);
        glds16(Vg + (size_t)key * 32 + chv * 8, lv + pc * 1024);
    }
    const int qrow = q0 + wid * 16 + fr;
    bf16x8 qf;
    asm volatile("global_load_dwordx4 %0, %1, off" : "=v"(qf) : "v"(Q + (size_t)qrow * 32 + fq * 8));
    asm volatile("s_waitcnt vmcnt(0)" : "+v"(qf) :: "memory");
    __builtin_amdgcn_s_barrier();
    const int kbase = q0 + wid * 16 - 256;
    const int istart = kbase < 0 ? (-kbase) >> 4 : 0;
    f32x4 s[17];
    const int koff = (wid * 16 + fr) * 64 + ((fq ^ ((-(fr >> 2)) & 3)) * 16);
#pragma unroll
    for (int i = 0; i < 17; ++i) {
        const bf16x8 kf = *(const LAS bf16x8*)(lk + koff + i * 1024);
        s[i] = __builtin_amdgcn_mfma_f32_16x16x32_bf16(kf, qf, (f32x4){0.f, 0.f, 0.f, 0.f}, 0, 0, 0);
    }
    asm volatile("s_nop 7\n\ts_nop 7\n\ts_nop 3" : "+v"(s[0]), "+v"(s[1]), "+v"(s[2]), "+v"(s[3]), "+v"(s[4]), "+v"(s[5]), "+v"(s[6]), "+v"(s[7]), "+v"(s[8]),
                 "+v"(s[9]), "+v"(s[10]), "+v"(s[11]), "+v"(s[12]), "+v"(s[13]), "+v"(s[14]), "+v"(s[15]), "+v"(s[16]));
    const float NEG = -3.0e38f;
#pragma unroll
    for (int r = 0; r < 4; ++r) { if (!(4 * fq + r > fr)) s[0][r] = NEG; if (!(4 * fq + r <= fr)) s[16][r] = NEG; }
    if (istart > 0) {
#pragma unroll
        for (int i = 0; i < 16; ++i) if (i < istart) s[i] = (f32x4){NEG, NEG, NEG, NEG};
    }
    float mx = NEG;
#pragma unroll
    for (int i = 0; i < 17; ++i) { asm("v_max3_f32 %0, %1, %2, %3" : "=v"(mx) : "v"(mx), "v"(s[i][0]), "v"(s[i][1]));
                                   asm("v_max3_f32 %0, %1, %2, %3" : "=v"(mx) : "v"(mx), "v"(s[i][2]), "v"(s[i][3])); }
    mx = fmaxf(mx, __shfl_xor(mx, 16)); mx = fmaxf(mx, __shfl_xor(mx, 32));
    const float c = 0.17677669529663687f * 1.4426950408889634f;
    const float mc = -mx * c;
    typedef float f32x2 __attribute__((ext_vector_type(2)));
    const f32x2 c2 = {c, c}, mc2 = {mc, mc};
#pragma unroll
    for (int i = 0; i < 17; ++i) {
        const f32x2 lo = __builtin_elementwise_fma((f32x2){s[i][0], s[i][1]}, c2, mc2), hi = __builtin_elementwise_fma((f32x2){s[i][2], s[i][3]}, c2, mc2);
        s[i][0] = __builtin_amdgcn_exp2f(lo.x); s[i][1] = __builtin_amdgcn_exp2f(lo.y); s[i][2] = __builtin_amdgcn_exp2f(hi.x); s[i][3] = __builtin_amdgcn_exp2f(hi.y);
    }
    f32x4 oacc[2] = {(f32x4){0.f, 0.f, 0.f, 0.f}, (f32x4){0.f, 0.f, 0.f, 0.f}};
    f32x4 osum = (f32x4){0.f, 0.f, 0.f, 0.f};
    const bf16x8 ones = (bf16x8){0x3F80, 0x3F80, 0x3F80, 0x3F80, 0x3F80, 0x3F80, 0x3F80, 0x3F80};
    const int q4 = (lane & 15) >> 2, p4 = lane & 3;
    const unsigned va0 = (unsigned)(size_t)lv + (wid * 16 + 4 * fq + q4) * 64 + p4 * 8 + ((fq & 1) << 5), va1 = va0 ^ 32;
    s16x4 vr[2][4];
#define V_READ(SET, cc) do { vr[SET][0] = tr_read_a<(cc) * 2048>(va0); vr[SET][1] = tr_read_a<(cc) * 2048 + ((cc) < 8 ? 1024 : 0)>(va0); \
        vr[SET][2] = tr_read_a<(cc) * 2048>(va1); vr[SET][3] = tr_read_a<(cc) * 2048 + ((cc) < 8 ? 1024 : 0)>(va1); } while (0)
#define V_STEP(cc) do { constexpr int cur = (cc) & 1; \
        if constexpr ((cc) < 8) { V_READ(cur ^ 1, (cc) + 1); asm volatile("s_waitcnt lgkmcnt(4)" : "+v"(vr[cur][0]), "+v"(vr[cur][1]), "+v"(vr[cur][2]), "+v"(vr[cur][3])); } \
        else asm volatile("s_waitcnt lgkmcnt(0)" : "+v"(vr[cur][0]), "+v"(vr[cur][1]), "+v"(vr[cur][2]), "+v"(vr[cur][3])); \
        u32x4 pw; pw.x = pk_bf16(s[2 * (cc)][0], s[2 * (cc)][1]); pw.y = pk_bf16(s[2 * (cc)][2], s[2 * (cc)][3]); \
        if constexpr ((cc) < 8) { pw.z = pk_bf16(s[2 * (cc) + 1][0], s[2 * (cc) + 1][1]); pw.w = pk_bf16(s[2 * (cc) + 1][2], s[2 * (cc) + 1][3]); } else { pw.z = 0u; pw.w = 0u; } \
        const bf16x8 pf = __builtin_bit_cast(bf16x8, pw); \
        osum = __builtin_amdgcn_mfma_f32_16x16x32_bf16(ones, pf, osum, 0, 0, 0); \
        oacc[0] = __builtin_amdgcn_mfma_f32_16x16x32_bf16(__builtin_shufflevector(vr[cur][0], vr[cur][1], 0, 1, 2, 3, 4, 5, 6, 7), pf, oacc[0], 0, 0, 0); \
        oacc[1] = __builtin_amdgcn_mfma_f32_16x16x32_bf16(__builtin_shufflevector(vr[cur][2], vr[cur][3], 0, 1, 2, 3, 4, 5, 6, 7), pf, oacc[1], 0, 0, 0); } while (0)
    V_READ(0, 0);
    V_STEP(0); V_STEP(1); V_STEP(2); V_STEP(3); V_STEP(4); V_STEP(5); V_STEP(6); V_STEP(7); V_STEP(8);
#undef V_READ
#undef V_STEP
    const float inv = 1.0f / osum[0];
#pragma unroll
    for (int dt = 0; dt < 2; ++dt) {
        u32x2 w; w.x = pk_bf16(oacc[dt][0] * inv, oacc[dt][1] * inv); w.y = pk_bf16(oacc[dt][2] * inv, oacc[dt][3] * inv);
        *(u32x2*)(o + (size_t)qrow * D_ + head * 32 + dt * 16 + 4 * fq) = w;
    }
}

extern "C" void kernel_launch(void* const* d_in, const int* in_sizes, int n_in, void* d_out, int out_size, void* d_ws, size_t ws_size,
                              hipStream_t stream) {
    const bf16_t* x = (const bf16_t*)d_in[0];
    const bf16_t* wqkv = (const bf16_t*)d_in[1];
    const bf16_t* wo = (const bf16_t*)d_in[2];
    const bf16_t* w1 = (const bf16_t*)d_in[4];
    const bf16_t* w3 = (const bf16_t*)d_in[5];
    const bf16_t* w2 = (const bf16_t*)d_in[6];
    bf16_t* out = (bf16_t*)d_out;
    char* ws = (char*)d_ws;
    bf16_t* xn = (bf16_t*)(ws);
    bf16_t* qkv = (bf16_t*)(ws + (size_t)4 * 1024 * 1024);
    bf16_t* o = (bf16_t*)(ws + (size_t)20 * 1024 * 1024);
    bf16_t* h = (bf16_t*)(ws + (size_t)24 * 1024 * 1024);
    bf16_t* hn = (bf16_t*)(ws + (size_t)28 * 1024 * 1024);
    bf16_t* act = (bf16_t*)(ws + (size_t)32 * 1024 * 1024);

    {
        auto kern = gemm_pp<128, 192, 2, 4, 3, EPI_QKV, 8, 4, 1024>;
        const int shm = 3 * (128 + 192) * 128;
        (void)hipFuncSetAttribute((const void*)kern, hipFuncAttributeMaxDynamicSharedMemorySize, shm);
        kern<<<256, 512, shm, stream>>>(QKV_LNF ? x : xn, D_, wqkv, 3 * D_, wo, qkv, 16);
    }
    attn_kernel<<<32 * (S_ / AT_QB), AT_NW * 64, 0, stream>>>(qkv, o);
    {
        auto kern = gemm_pp<128, 64, 4, 2, 6, EPI_RES, 4, 8, 1024>;
        const int shm = 6 * (128 + 64) * 128;
        (void)hipFuncSetAttribute((const void*)kern, hipFuncAttributeMaxDynamicSharedMemorySize, shm);
        kern<<<256, 512, shm, stream>>>(o, D_, wo, D_, x, h, 16);
    }
    ln_kernel<<<S_ / 4, 256, 0, stream>>>(h, hn, (const unsigned*)w2, 0u, (const unsigned*)w2, 0u);
    {
        const int shm = 8 * 16384;
        (void)hipFuncSetAttribute((const void*)gemm_gateup, hipFuncAttributeMaxDynamicSharedMemorySize, shm);
        gemm_gateup<<<256, 512, shm, stream>>>(hn, w1, w3, act, (const unsigned*)w2);
    }
    {
        auto kern = gemm_pp<128, 64, 4, 2, 6, EPI_RES, 4, 8, 3584>;
        const int shm = 6 * (128 + 64) * 128;
        (void)hipFuncSetAttribute((const void*)kern, hipFuncAttributeMaxDynamicSharedMemorySize, shm);
        kern<<<256, 512, shm, stream>>>(act, F_, w2, D_, h, out, 16);
    }
}
```

```cpp
#include <hip/hip_runtime.h>
#include <hip/hip_bf16.h>
#include <stdint.h>

typedef unsigned short bf16_t;
typedef short bf16x8 __attribute__((ext_vector_type(8)));
typedef short s16x4 __attribute__((ext_vector_type(4)));
typedef float f32x4 __attribute__((ext_vector_type(4)));
typedef unsigned u32x4 __attribute__((ext_vector_type(4)));
typedef unsigned u32x2 __attribute__((ext_vector_type(2)));
#define LAS __attribute__((address_space(3)))
#define DI __device__ __forceinline__

constexpr int S_ = 2048, D_ = 1024, F_ = 3584, H_ = 32, DH_ = 32, WIN_ = 256;

DI float bf2f(unsigned short b) { return __uint_as_float(((unsigned)b) << 16); }
DI unsigned pk_bf16(float lo, float hi) {
    typedef __bf16 bf2 __attribute__((ext_vector_type(2)));
    bf2 v; v.x = (__bf16)lo; v.y = (__bf16)hi;
    return __builtin_bit_cast(unsigned, v);
}
DI s16x4 tr_read(LAS unsigned char* p) { return __builtin_amdgcn_ds_read_tr16_b64_v4i16((LAS s16x4*)p); }
template <int OFF> DI s16x4 tr_read_a(unsigned addr) { s16x4 r; asm volatile("ds_read_b64_tr_b16 %0, %1 offset:%2" : "=v"(r) : "v"(addr), "i"(OFF)); return r; }
DI void glds16(const void* g, LAS unsigned char* l) {
    __builtin_amdgcn_global_load_lds((const __attribute__((address_space(1))) unsigned*)g, (LAS unsigned*)l, 16, 0, 0);
}
#define WAIT_V(n) asm volatile("s_waitcnt vmcnt(" #n ")" ::: "memory")
#define WAIT_L(n) asm volatile("s_waitcnt lgkmcnt(" #n ")" ::: "memory")
#define BAR __builtin_amdgcn_s_barrier()
#define SCHED __builtin_amdgcn_sched_barrier(0)
#define STAMP_DECL(kid)
#define STAMP_AT()
#define STAMP_RT()
#define STAMP_RT2()

__global__ __launch_bounds__(256) void ln_kernel(const bf16_t* __restrict__ in, bf16_t* __restrict__ out, const unsigned* __restrict__ pf, unsigned pf_lines,
                                                 const unsigned* __restrict__ pfb, unsigned pfb_lines) {
    const int row = blockIdx.x * 4 + (threadIdx.x >> 6), lane = threadIdx.x & 63;
    const u32x4* p = (const u32x4*)(in + (size_t)row * D_);
    u32x4 a = p[lane], b = p[lane + 64];
    unsigned pfv = 0;
    { const unsigned li = blockIdx.x * 256u + threadIdx.x; if (li < pf_lines) pfv = pf[(size_t)li * 32];
      else if (li - pf_lines < pfb_lines) pfv = pfb[(size_t)(li - pf_lines) * 32]; }
    float v[16];
#pragma unroll
    for (int i = 0; i < 4; ++i) { v[2 * i] = __uint_as_float(a[i] << 16); v[2 * i + 1] = __uint_as_float(a[i] & 0xffff0000u);
                                  v[8 + 2 * i] = __uint_as_float(b[i] << 16); v[8 + 2 * i + 1] = __uint_as_float(b[i] & 0xffff0000u); }
    float s = 0.f;
#pragma unroll
    for (int i = 0; i < 16; ++i) s += v[i];
#pragma unroll
    for (int o = 32; o >= 1; o >>= 1) s += __shfl_xor(s, o);
    const float mean = s * (1.0f / D_);
    float q = 0.f;
#pragma unroll
    for (int i = 0; i < 16; ++i) { v[i] -= mean; q += v[i] * v[i]; }
#pragma unroll
    for (int o = 32; o >= 1; o >>= 1) q += __shfl_xor(q, o);
    const float rstd = 1.0f / sqrtf(q * (1.0f / D_) + 1e-5f);
    u32x4 oa, ob;
#pragma unroll
    for (int i = 0; i < 4; ++i) { oa[i] = pk_bf16(v[2 * i] * rstd, v[2 * i + 1] * rstd); ob[i] = pk_bf16(v[8 + 2 * i] * rstd, v[8 + 2 * i + 1] * rstd); }
    u32x4* po = (u32x4*)(out + (size_t)row * D_);
    po[lane] = oa; po[lane + 64] = ob;
    asm volatile("" :: "v"(pfv));
}

DI int bx_of_k(int k) { return ((k >> 1) & 1) | (((k >> 3) & 1) << 1); }

enum { EPI_QKV = 0, EPI_RES = 1, EPI_WO = 2 };
#define QKV_LNF 1
DI u32x4 pair_swap(u32x2 t0, u32x2 t1) {
    auto a = __builtin_amdgcn_permlane16_swap(t0.x, t1.x, false, false);
    auto b = __builtin_amdgcn_permlane16_swap(t0.y, t1.y, false, false);
    return (u32x4){a[0], b[0], a[1], b[1]};
}

template <int N> DI void wait_v() { asm volatile("s_waitcnt vmcnt(%0)" :: "i"(N) : "memory"); }
template <int OFF> DI bf16x8 ld128_a(unsigned addr) { bf16x8 r; asm volatile("ds_read_b128 %0, %1 offset:%2" : "=v"(r) : "v"(addr), "i"(OFF)); return r; }
typedef __bf16 bf2_t __attribute__((ext_vector_type(2)));
DI float dot2_one(unsigned w, float c) { return __builtin_amdgcn_fdot2_f32_bf16(__builtin_bit_cast(bf2_t, w), __builtin_bit_cast(bf2_t, 0x3F803F80u), c, false); }
DI float dot2_sq(unsigned w, float c) { return __builtin_amdgcn_fdot2_f32_bf16(__builtin_bit_cast(bf2_t, w), __builtin_bit_cast(bf2_t, w), c, false); }
DI void row_stats_acc(const bf16x8& f, float& rs, float& rq) { const u32x4 w = __builtin_bit_cast(u32x4, f);
#pragma unroll
    for (int i = 0; i < 4; ++i) { rs = dot2_one(w[i], rs); rq = dot2_sq(w[i], rq); } }
DI void col_sum_acc(const s16x4& lo, const s16x4& hi, float& cs) { const u32x2 a = __builtin_bit_cast(u32x2, lo), b = __builtin_bit_cast(u32x2, hi);
    cs = dot2_one(a.x, cs); cs = dot2_one(a.y, cs); cs = dot2_one(b.x, cs); cs = dot2_one(b.y, cs); }
DI float red_fq(float v) { v += __shfl_xor(v, 16); v += __shfl_xor(v, 32); return v; }

template <int BM, int BN, int WM, int WN, int NBUF, int EPI, int RM, int RN, int K>
__global__ __launch_bounds__(512, 2) void gemm_pp(const bf16_t* __restrict__ A, int lda, const bf16_t* __restrict__ W, int ldb,
                                                   const bf16_t* __restrict__ res, bf16_t* __restrict__ out, int tiles_m,
                                                   float* __restrict__ meanx, bf16_t* __restrict__ wbar, bf16_t* __restrict__ hc, float* __restrict__ sqpart) {
    extern __shared__ __attribute__((aligned(16))) unsigned char smem_raw[];
    LAS unsigned char* lds = (LAS unsigned char*)smem_raw;
    constexpr int MT = BM / WM / 16, NT = BN / WN / 16;
    constexpr int NPA = BM / 64, NPB = BN / 64, NP = NPA + NPB;
    constexpr int ABYTES = BM * 128, TILE_BYTES = (BM + BN) * 128;
    constexpr int nt = K / 64;
    constexpr bool LNF = (EPI == EPI_QKV) && QKV_LNF;
    static_assert(WM * WN == 8, "8 waves");
    static_assert(MT == 2 || MT == 4, "MT");
    static_assert(!LNF || (MT == 4 && WN == 4 && WM == 2), "LN fold geometry");
    STAMP_DECL(EPI == EPI_QKV ? 0 : (K == 1024 ? 1 : 2)); STAMP_RT(); STAMP_AT();
    const int tid = threadIdx.x, wid = __builtin_amdgcn_readfirstlane(tid >> 6), lane = tid & 63;
    const int wm = wid / WN, wn = wid % WN, fr = lane & 15, fq = lane >> 4;
    int tm, tn;
    { const int bid = blockIdx.x, xcd = bid & 7, idx = bid >> 3; const int regm = tiles_m / RM;
      const int rr = xcd % regm, rc = xcd / regm; tm = rr * RM + (idx % RM); tn = rc * RN + (idx / RM); }
    const int m0 = tm * BM, n0 = tn * BN;
    const bf16_t* asrc[NPA]; const bf16_t* bsrc[NPB];
#pragma unroll
    for (int i = 0; i < NPA; ++i) { const int row = (wid + 8 * i) * 8 + (lane >> 3), ch = (lane & 7) ^ ((row >> 1) & 7);
        asrc[i] = A + (size_t)(m0 + row) * lda + ch * 8; }
#pragma unroll
    for (int j = 0; j < NPB; ++j) { const int k = wid * 8 + (lane >> 3), s16 = lane & 7, n16 = (s16 >> 1) ^ bx_of_k(k);
        bsrc[j] = W + (size_t)k * ldb + n0 + j * 64 + n16 * 16 + (s16 & 1) * 8; }
    const size_t bstep = (size_t)64 * ldb;
    auto stage = [&](int t, int buf) {
        LAS unsigned char* base = lds + buf * TILE_BYTES + wid * 1024;
#pragma unroll
        for (int i = 0; i < NPA; ++i) glds16(asrc[i] + (size_t)t * 64, base + i * 8192);
#pragma unroll
        for (int j = 0; j < NPB; ++j) glds16(bsrc[j] + (size_t)t * bstep, base + ABYTES + j * 8192);
    };
    constexpr bool ROT = LNF || (EPI == EPI_WO);
    static_assert(!ROT || MT == WN, "rotation geometry");
    const int rotm = ROT ? wn : 0, krot = LNF ? wm : 0;
    const int sw = (fr >> 1) & 7;
    unsigned aadr[MT][2], badr[NT][2];
#pragma unroll
    for (int j = 0; j < MT; ++j)
#pragma unroll
        for (int sl = 0; sl < 2; ++sl) aadr[j][sl] = (unsigned)(size_t)lds + (wm * MT * 16 + ((j + rotm) & (MT - 1)) * 16 + fr) * 128 + (((((sl ^ krot) * 4) + fq) ^ sw) * 16);
    { const int q = (lane & 15) >> 2, p = lane & 3, xl = ((q >> 1) & 1) | ((fq & 1) << 1);
#pragma unroll
      for (int n = 0; n < NT; ++n) { const int ntg = wn * NT + n;
          const unsigned b0 = (unsigned)(size_t)lds + ABYTES + (ntg >> 2) * 8192 + (8 * fq + q) * 128 + (((ntg & 3) ^ xl) * 32) + p * 8;
          badr[n][0] = b0 + krot * 4096; badr[n][1] = b0 + (1 - krot) * 4096; } }
    f32x4 acc[MT][NT];
#pragma unroll
    for (int m = 0; m < MT; ++m)
#pragma unroll
        for (int n = 0; n < NT; ++n) acc[m][n] = (f32x4){0.f, 0.f, 0.f, 0.f};
    f32x4 rsacc = (f32x4){0.f, 0.f, 0.f, 0.f}, rqacc = rsacc, csacc[NT];
#pragma unroll
    for (int n = 0; n < NT; ++n) csacc[n] = rsacc;
    const bf16x8 ones = (bf16x8){0x3F80, 0x3F80, 0x3F80, 0x3F80, 0x3F80, 0x3F80, 0x3F80, 0x3F80};
    bf16x8 afA[MT][2], afB[MT][2]; s16x4 bA[NT][4], bB[NT][4];
    constexpr bool CEN = (EPI == EPI_WO);
    bf16x8 wbA[2], wbB[2]; f32x4 macc = (f32x4){0.f, 0.f, 0.f, 0.f};
    const unsigned wbadr = (unsigned)(size_t)lds + NBUF * TILE_BYTES + fq * 16;
#define P_READ(AF, BR, WB, buf, tt) do { const unsigned bo_ = (unsigned)((buf) * TILE_BYTES); \
        if constexpr (CEN) { WB[0] = ld128_a<0>(wbadr + (tt) * 128); WB[1] = ld128_a<64>(wbadr + (tt) * 128); } \
        if constexpr (ROT) { \
            _Pragma("unroll") for (int n = 0; n < NT; ++n) { const unsigned b0_ = badr[n][0] + bo_, b1_ = badr[n][1] + bo_; \
                BR[n][0] = tr_read_a<0>(b0_); BR[n][1] = tr_read_a<512>(b0_); BR[n][2] = tr_read_a<0>(b1_); BR[n][3] = tr_read_a<512>(b1_); } \
            _Pragma("unroll") for (int j = 0; j < MT; ++j) { AF[j][0] = ld128_a<0>(aadr[j][0] + bo_); AF[j][1] = ld128_a<0>(aadr[j][1] + bo_); } \
        } else { \
            _Pragma("unroll") for (int n = 0; n < NT; ++n) { const unsigned ba = badr[n][0] + bo_; \
                BR[n][0] = tr_read_a<0>(ba); BR[n][1] = tr_read_a<512>(ba); BR[n][2] = tr_read_a<4096>(ba); BR[n][3] = tr_read_a<4608>(ba); } \
            const unsigned a0_ = aadr[0][0] + bo_, a1_ = aadr[0][1] + bo_; \
            AF[0][0] = ld128_a<0>(a0_); AF[0][1] = ld128_a<0>(a1_); AF[1][0] = ld128_a<2048>(a0_); AF[1][1] = ld128_a<2048>(a1_); \
            if constexpr (MT == 4) { AF[MT - 2][0] = ld128_a<4096>(a0_); AF[MT - 2][1] = ld128_a<4096>(a1_); AF[MT - 1][0] = ld128_a<6144>(a0_); AF[MT - 1][1] = ld128_a<6144>(a1_); } } } while (0)
#define P_WAITL(AF, BR, WB) do { _Pragma("unroll") for (int n = 0; n < NT; ++n) \
        asm volatile("s_waitcnt lgkmcnt(0)" : "+v"(BR[n][0]), "+v"(BR[n][1]), "+v"(BR[n][2]), "+v"(BR[n][3]) :: "memory"); \
        _Pragma("unroll") for (int m = 0; m < MT; ++m) asm volatile("" : "+v"(AF[m][0]), "+v"(AF[m][1])); \
        if constexpr (CEN) asm volatile("" : "+v"(WB[0]), "+v"(WB[1])); } while (0)
#define P_MMA(AF, BR, WB) do { \
        if constexpr (CEN) { _Pragma("unroll") for (int kk = 0; kk < 2; ++kk) macc = __builtin_amdgcn_mfma_f32_16x16x32_bf16(WB[kk], AF[0][kk], macc, 0, 0, 0); } \
         _Pragma("unroll") for (int kk = 0; kk < 2; ++kk) _Pragma("unroll") for (int m = 0; m < MT; ++m) _Pragma("unroll") for (int n = 0; n < NT; ++n) \
        acc[m][n] = __builtin_amdgcn_mfma_f32_16x16x32_bf16(__builtin_shufflevector(BR[n][2 * kk], BR[n][2 * kk + 1], 0, 1, 2, 3, 4, 5, 6, 7), AF[m][kk], acc[m][n], 0, 0, 0); \
        if constexpr (LNF) { _Pragma("unroll") for (int kk = 0; kk < 2; ++kk) { rsacc = __builtin_amdgcn_mfma_f32_16x16x32_bf16(ones, AF[0][kk], rsacc, 0, 0, 0); \
                rqacc = __builtin_amdgcn_mfma_f32_16x16x32_bf16(AF[0][kk], AF[0][kk], rqacc, 0, 0, 0); } \
            _Pragma("unroll") for (int n = 0; n < NT; ++n) csacc[n] = __builtin_amdgcn_mfma_f32_16x16x32_bf16(__builtin_shufflevector(BR[n][0], BR[n][1], 0, 1, 2, 3, 4, 5, 6, 7), ones, csacc[n], 0, 0, 0); } } while (0)
#define P_STEP(AFC, BRC, WBC, AFN, BRN, WBN, FULL, WN_, RESLD) do { \
        P_WAITL(AFC, BRC, WBC); \
        wait_v<WN_>(); \
        BAR; \
        const int nb_ = (buf + 1 == NBUF) ? 0 : buf + 1; \
        if (FULL || t + 1 < nt) P_READ(AFN, BRN, WBN, nb_, t + 1); \
        if (FULL || t + NBUF < nt) stage(t + NBUF, buf); \
        if (RESLD) { _Pragma("unroll") for (int m = 0; m < MT; ++m) _Pragma("unroll") for (int n = 0; n < NT; ++n) \
            rres[m][n] = *(const u32x2*)(res + (size_t)(m0 + (wm * MT + ((m + rotm) & (MT - 1))) * 16 + fr) * D_ + n0 + (wn * NT + n) * 16 + 4 * fq); \
            if constexpr (CEN) mxr = meanx[m0 + (wm * MT + rotm) * 16 + fr]; } \
        if constexpr (PIN_ORDER) SCHED; \
        P_MMA(AFC, BRC, WBC); \
        buf = nb_; ++t; } while (0)
    constexpr bool PIN_ORDER = (EPI == EPI_WO);
    u32x2 rres[MT][NT]; float mxr;
    constexpr int NR = (EPI != EPI_QKV) ? MT * NT + (CEN ? 1 : 0) : 0;
    if constexpr (CEN) { if (wid < 2) glds16(wbar + wid * 512 + lane * 8, lds + NBUF * TILE_BYTES + wid * 1024); }
#pragma unroll
    for (int t = 0; t < NBUF; ++t) stage(t, t);
    wait_v<(NBUF - 1) * NP>();
    BAR;
    STAMP_AT();
    P_READ(afA, bA, wbA, 0, 0);
    int buf = 0, t = 0;
    constexpr int nmain = ((nt - NBUF) / 2) * 2;
    for (; t < nmain;) {
        P_STEP(afA, bA, wbA, afB, bB, wbB, true, (NBUF - 2) * NP, false);
        P_STEP(afB, bB, wbB, afA, bA, wbA, true, (NBUF - 2) * NP, false);
    }
#define P_TAILW(tt) ((((tt) + NBUF - 1 < nt - 1 ? (tt) + NBUF - 1 : nt - 1) - ((tt) + 1) > 0 ? (((tt) + NBUF - 1 < nt - 1 ? (tt) + NBUF - 1 : nt - 1) - ((tt) + 1)) : 0) * NP + ((tt) > nt - NBUF ? NR : 0))
#define P_TAIL2(tt) do { t = (tt); P_STEP(afA, bA, wbA, afB, bB, wbB, false, P_TAILW(tt), (NR > 0 && (tt) == nt - NBUF)); \
        P_STEP(afB, bB, wbB, afA, bA, wbA, false, P_TAILW((tt) + 1), (NR > 0 && (tt) + 1 == nt - NBUF)); } while (0)
    if constexpr (nmain + 0 < nt) P_TAIL2(nmain + 0);
    if constexpr (nmain + 2 < nt) P_TAIL2(nmain + 2);
    if constexpr (nmain + 4 < nt) P_TAIL2(nmain + 4);
    if constexpr (nmain + 6 < nt) P_TAIL2(nmain + 6);
    static_assert(nmain + 8 >= nt, "tail");
#undef P_TAILW
#undef P_TAIL2
    STAMP_AT();
#undef P_READ
#undef P_WAITL
#undef P_MMA
#undef P_STEP
    if constexpr (EPI != EPI_QKV) {
#pragma unroll
        for (int m = 0; m < MT; ++m) {
#pragma unroll
            for (int n = 0; n < NT; ++n) asm volatile("" : "+v"(rres[m][n])); }
        if constexpr (CEN) asm volatile("" : "+v"(mxr));
    }
    typedef float f32x2 __attribute__((ext_vector_type(2)));
    LAS f32x2* stat = (LAS f32x2*)lds;
    LAS float* cpart = (LAS float*)(lds + BM * 8);
    if constexpr (LNF) {
        const float rq = (fr & 2) ? ((fr & 1) ? rqacc[3] : rqacc[2]) : ((fr & 1) ? rqacc[1] : rqacc[0]);
        const float mean = rsacc[0] * (1.0f / K), var = fmaxf(rq * (1.0f / K) - mean * mean, 0.f);
        if (fq == (fr >> 2)) { stat[wm * 64 + wn * 16 + fr] = (f32x2){mean, 1.0f / sqrtf(var + 1e-5f)}; if (tn == 0) meanx[m0 + wm * 64 + wn * 16 + fr] = mean; }
#pragma unroll
        for (int n = 0; n < NT; ++n) if (fr == 0) *(LAS f32x4*)(cpart + wm * BN + (wn * NT + n) * 16 + 4 * fq) = csacc[n];
        __syncthreads();
    }
    if constexpr (CEN) { if (fq == 0) stat[(wm * MT + rotm) * 16 + fr] = (f32x2){mxr + macc[0], 0.f}; __syncthreads(); }
    u32x4 wo0 = (u32x4){0u, 0u, 0u, 0u}, wo1 = wo0;
    const int worow = blockIdx.x * 8 + wid;
    if constexpr (EPI == EPI_QKV) { if (worow < D_) { const u32x4* pw = (const u32x4*)(res + (size_t)worow * D_); wo0 = pw[lane]; wo1 = pw[lane + 64]; } }
    const int pcol = (fq & 1) ? 16 + 4 * (fq - 1) : 4 * fq;
    f32x4 csum[NT];
    if constexpr (LNF) {
#pragma unroll
        for (int n = 0; n < NT; ++n) csum[n] = *(const LAS f32x4*)(cpart + (wn * NT + n) * 16 + 4 * fq) + *(const LAS f32x4*)(cpart + BN + (wn * NT + n) * 16 + 4 * fq);
    }
#pragma unroll
    for (int m = 0; m < MT; ++m) {
        const int lrow = (wm * MT + ((m + rotm) & (MT - 1))) * 16 + fr, row = m0 + lrow;
        f32x2 st; if constexpr (LNF || CEN) st = stat[lrow];
        u32x2 w[NT], wc[NT];
#pragma unroll
        for (int n = 0; n < NT; ++n) {
            f32x4 v = acc[m][n];
            if constexpr (LNF) v = (v - csum[n] * st.x) * st.y;
            if constexpr (EPI != EPI_QKV) { const u32x2 r = rres[m][n];
                v[0] += __uint_as_float(r.x << 16); v[1] += __uint_as_float(r.x & 0xffff0000u);
                v[2] += __uint_as_float(r.y << 16); v[3] += __uint_as_float(r.y & 0xffff0000u); }
            w[n].x = pk_bf16(v[0], v[1]); w[n].y = pk_bf16(v[2], v[3]);
            if constexpr (CEN) { v -= st.x; wc[n].x = pk_bf16(v[0], v[1]); wc[n].y = pk_bf16(v[2], v[3]); }
        }
        if constexpr (CEN) {
            static_assert(NT == 2, "centred epilogue geometry");
            float q = 0.f;
#pragma unroll
            for (int n = 0; n < 2; ++n) { const float a = __uint_as_float(wc[n].x << 16), b = __uint_as_float(wc[n].x & 0xffff0000u),
                c = __uint_as_float(wc[n].y << 16), d = __uint_as_float(wc[n].y & 0xffff0000u); q += (a * a + b * b) + (c * c + d * d); }
            q = red_fq(q);
            if (fq == 0) cpart[wn * BM + lrow] = q;
            *(u32x4*)(hc + (size_t)row * D_ + n0 + wn * 32 + pcol) = pair_swap(wc[0], wc[1]);
        }
#pragma unroll
        for (int n = 0; n + 1 < NT; n += 2) {
            const u32x4 o = pair_swap(w[n], w[n + 1]);
            const int col = n0 + (wn * NT + n) * 16 + pcol;
            if constexpr (EPI == EPI_QKV) *(u32x4*)(out + ((size_t)(col >> 5) * S_ + row) * 32 + (col & 31)) = o;
            else *(u32x4*)(out + (size_t)row * D_ + col) = o;
        }
        if constexpr (NT & 1) {
            const int col = n0 + (wn * NT + NT - 1) * 16 + 4 * fq;
            if constexpr (EPI == EPI_QKV) *(u32x2*)(out + ((size_t)(col >> 5) * S_ + row) * 32 + (col & 31)) = w[NT - 1];
            else *(u32x2*)(out + (size_t)row * D_ + col) = w[NT - 1];
        }
    }
    if constexpr (CEN) { __syncthreads(); if (tid < BM) sqpart[(size_t)tn * S_ + m0 + tid] = cpart[tid] + cpart[BM + tid]; }
    if constexpr (EPI == EPI_QKV) {
        float sm = 0.f;
#pragma unroll
        for (int i = 0; i < 4; ++i) sm += (__uint_as_float(wo0[i] << 16) + __uint_as_float(wo0[i] & 0xffff0000u)) + (__uint_as_float(wo1[i] << 16) + __uint_as_float(wo1[i] & 0xffff0000u));
#pragma unroll
        for (int o = 32; o >= 1; o >>= 1) sm += __shfl_xor(sm, o);
        if (lane == 0 && worow < D_) wbar[worow] = (bf16_t)(pk_bf16(sm * (1.0f / D_), 0.f) & 0xffffu);
    }
    WAIT_V(0); STAMP_AT(); STAMP_RT2();
}

__global__ __launch_bounds__(512, 2) void gemm_gateup(const bf16_t* __restrict__ A, const bf16_t* __restrict__ W1, const bf16_t* __restrict__ W3,
                                                       bf16_t* __restrict__ act, const unsigned* __restrict__ pf, const float* __restrict__ sqpart) {
    extern __shared__ __attribute__((aligned(16))) unsigned char smem_raw[];
    LAS unsigned char* lds = (LAS unsigned char*)smem_raw;
    constexpr int K = D_, HTB = 16384;
    STAMP_DECL(3); STAMP_RT(); STAMP_AT();
    const int tid = threadIdx.x, wid = __builtin_amdgcn_readfirstlane(tid >> 6), lane = tid & 63;
    const int wr = wid >> 2, wc = wid & 3, fr = lane & 15, fq = lane >> 4;
    if (blockIdx.x >= 224) {
        constexpr unsigned NL = (unsigned)((size_t)F_ * D_ * 2 / 128);
        const unsigned l0 = (blockIdx.x - 224) * 512u + threadIdx.x;
        unsigned a = 0;
#pragma unroll
        for (int i = 0; i < 4; ++i) { const unsigned li = l0 + i * 16384u; if (li < NL) a += ((const unsigned*)W1)[(size_t)li * 32] + ((const unsigned*)W3)[(size_t)li * 32]; }
#pragma unroll
        for (int i = 0; i < 4; ++i) { const unsigned li = l0 + i * 16384u; if (li < NL) a += pf[(size_t)li * 32]; }
        asm volatile("" :: "v"(a));
        return;
    }
    int tm, tn;
    { const int bid = blockIdx.x, xcd = bid & 7, idx = bid >> 3; tm = (xcd & 1) * 4 + (idx & 3); tn = (xcd >> 1) * 7 + (idx >> 2); }
    const int m0 = tm * 256, f0 = tn * 128;
    constexpr int nt = K / 64;
    unsigned aoffs[2], boffs[2];
#pragma unroll
    for (int i = 0; i < 2; ++i) { const int row = (wid + 8 * i) * 8 + (lane >> 3), ch = (lane & 7) ^ ((row >> 1) & 7);
        aoffs[i] = (unsigned)(row * K + ch * 8) * 2u;
        const int k = wid * 8 + (lane >> 3), s16 = lane & 7, n16 = (s16 >> 1) ^ bx_of_k(k);
        boffs[i] = (unsigned)(k * F_ + i * 64 + n16 * 16 + (s16 & 1) * 8) * 2u; }
    const char* gA0 = (const char*)(A + (size_t)m0 * K); const char* gA1 = gA0 + (size_t)128 * K * 2;
    const char* gB0 = (const char*)(W1 + f0); const char* gB1 = (const char*)(W3 + f0);
    constexpr size_t akstep = 128, bkstep = (size_t)64 * F_ * 2;
    const unsigned ldsw = (unsigned)wid * 1024u;
#define G_SA(b, h) (((b) * 2 + (h)) * HTB)
#define G_SB(b, h) ((4 + (b) * 2 + (h)) * HTB)
#define G_STAGE(bufoff, gbase, voff) do { _Pragma("unroll") for (int _i = 0; _i < 2; ++_i) glds16((gbase) + (voff)[_i], lds + (bufoff) + ldsw + _i * 8192); } while (0)
    const int sw = (fr >> 1) & 7;
    const int aoff0 = (wr * 64 + fr) * 128 + (((wr * 4 + fq) ^ sw) * 16), aoff1 = aoff0 ^ 64;
    unsigned badr[2][2];
    { const int q = (lane & 15) >> 2, p = lane & 3, xl = ((q >> 1) & 1) | ((fq & 1) << 1);
#pragma unroll
      for (int n = 0; n < 2; ++n) { const int ntg = wc * 2 + n;
          const unsigned b0 = (unsigned)(size_t)lds + 4 * HTB + (ntg >> 2) * 8192 + (8 * fq + q) * 128 + (((ntg & 3) ^ xl) * 32) + p * 8;
          badr[n][0] = b0 + wr * 4096; badr[n][1] = b0 + (1 - wr) * 4096; } }
#define G_LDA(dst, b, h) do { _Pragma("unroll") for (int m = 0; m < 4; ++m) { dst[m][0] = *(const LAS bf16x8*)(lds + G_SA(b, h) + aoff0 + m * 2048); \
        dst[m][1] = *(const LAS bf16x8*)(lds + G_SA(b, h) + aoff1 + m * 2048); } } while (0)
#define G_LDB(dst, bo, h) do { _Pragma("unroll") for (int n = 0; n < 2; ++n) { const unsigned b0_ = badr[n][0] + (bo), b1_ = badr[n][1] + (bo); \
        dst[n][0] = tr_read_a<(h) * HTB>(b0_); dst[n][1] = tr_read_a<(h) * HTB + 512>(b0_); \
        dst[n][2] = tr_read_a<(h) * HTB>(b1_); dst[n][3] = tr_read_a<(h) * HTB + 512>(b1_); } } while (0)
#define G_WAITB(R) do { asm volatile("s_waitcnt lgkmcnt(0)" : "+v"(R[0][0]), "+v"(R[0][1]), "+v"(R[0][2]), "+v"(R[0][3]), "+v"(R[1][0]), "+v"(R[1][1]), "+v"(R[1][2]), "+v"(R[1][3]) :: "memory"); } while (0)
#define G_FR(R, n, k) __builtin_shufflevector(R[n][2 * (k)], R[n][2 * (k) + 1], 0, 1, 2, 3, 4, 5, 6, 7)
#define G_MMA(ai, bj, At, Bt) do { __builtin_amdgcn_s_setprio(1); _Pragma("unroll") for (int m = 0; m < 4; ++m) _Pragma("unroll") for (int n = 0; n < 2; ++n) \
        _Pragma("unroll") for (int k = 0; k < 2; ++k) acc[ai][bj][m][n] = __builtin_amdgcn_mfma_f32_16x16x32_bf16(G_FR(Bt, n, k), At[m][k], acc[ai][bj][m][n], 0, 0, 0); \
        __builtin_amdgcn_s_setprio(0); } while (0)
    f32x4 acc[2][2][4][2];
#pragma unroll
    for (int a = 0; a < 2; ++a)
#pragma unroll
        for (int b = 0; b < 2; ++b)
#pragma unroll
            for (int m = 0; m < 4; ++m)
#pragma unroll
                for (int n = 0; n < 2; ++n) acc[a][b][m][n] = (f32x4){0.f, 0.f, 0.f, 0.f};
    bf16x8 At[4][2]; s16x4 B0[2][4], B1[2][4];
    G_STAGE(G_SB(0, 0), gB0, boffs); G_STAGE(G_SA(0, 0), gA0, aoffs); G_STAGE(G_SB(0, 1), gB1, boffs); G_STAGE(G_SA(0, 1), gA1, aoffs);
    if (wr == 1) BAR;
    WAIT_V(4); BAR;
    G_STAGE(G_SB(1, 0), gB0 + bkstep, boffs); G_STAGE(G_SA(1, 0), gA0 + akstep, aoffs); G_STAGE(G_SB(1, 1), gB1 + bkstep, boffs);
    WAIT_V(6); BAR;
    STAMP_AT();
    float sq[8];
    for (int t = 0; t < nt; t += 2) {
        const int t1 = t + 1, t2 = (t + 2 < nt) ? t + 2 : nt - 1, t3 = (t + 3 < nt) ? t + 3 : nt - 1;
        if (t == nt - 2) { const float* sp = sqpart + (size_t)((tid >> 8) * 8) * S_ + m0 + (tid & 255);
#pragma unroll
            for (int i = 0; i < 8; ++i) asm volatile("global_load_dword %0, %1, off" : "=v"(sq[i]) : "v"(sp + (size_t)i * S_)); }
        G_LDB(B0, 0, 0); SCHED; G_LDA(At, 0, 0); G_STAGE(G_SA(1, 1), gA1 + t1 * akstep, aoffs);
        WAIT_L(8); BAR; G_WAITB(B0); G_MMA(0, 0, At, B0); BAR; SCHED;
        G_LDB(B1, 0, 1); G_STAGE(G_SB(0, 0), gB0 + t2 * bkstep, boffs);
        BAR; G_WAITB(B1); G_MMA(0, 1, At, B1); BAR;
        G_LDA(At, 0, 1); G_STAGE(G_SA(0, 0), gA0 + t2 * akstep, aoffs);
        BAR; WAIT_L(0); G_MMA(1, 0, At, B0); BAR; SCHED;
        G_STAGE(G_SB(0, 1), gB1 + t2 * bkstep, boffs);
        WAIT_V(6); BAR; G_MMA(1, 1, At, B1); BAR;
        G_LDB(B0, 2 * HTB, 0); SCHED; G_LDA(At, 1, 0); G_STAGE(G_SA(0, 1), gA1 + t2 * akstep, aoffs);
        WAIT_L(8); BAR; G_WAITB(B0); G_MMA(0, 0, At, B0); BAR; SCHED;
        G_LDB(B1, 2 * HTB, 1); G_STAGE(G_SB(1, 0), gB0 + t3 * bkstep, boffs);
        BAR; G_WAITB(B1); G_MMA(0, 1, At, B1); BAR;
        G_LDA(At, 1, 1); G_STAGE(G_SA(1, 0), gA0 + t3 * akstep, aoffs);
        BAR; WAIT_L(0); G_MMA(1, 0, At, B0); BAR; SCHED;
        G_STAGE(G_SB(1, 1), gB1 + t3 * bkstep, boffs);
        WAIT_V(6); BAR; G_MMA(1, 1, At, B1); BAR;
    }
    STAMP_AT();
    asm volatile("s_waitcnt vmcnt(0)" : "+v"(sq[0]), "+v"(sq[1]), "+v"(sq[2]), "+v"(sq[3]), "+v"(sq[4]), "+v"(sq[5]), "+v"(sq[6]), "+v"(sq[7]) :: "memory");
    LAS float* sqs = (LAS float*)(lds + 8 * HTB);
    sqs[tid] = ((sq[0] + sq[1]) + (sq[2] + sq[3])) + ((sq[4] + sq[5]) + (sq[6] + sq[7]));
    if (wr == 0) BAR;
    __syncthreads();
    const int pcol = (fq & 1) ? 16 + 4 * (fq - 1) : 4 * fq;
#pragma unroll
    for (int ai = 0; ai < 2; ++ai)
#pragma unroll
        for (int m = 0; m < 4; ++m) {
            const int lrow = ai * 128 + wr * 64 + m * 16 + fr, row = m0 + lrow;
            const float rstd = 1.0f / sqrtf((sqs[lrow] + sqs[256 + lrow]) * (1.0f / K) + 1e-5f);
            u32x2 w[2];
#pragma unroll
            for (int n = 0; n < 2; ++n) {
                const f32x4 g = acc[ai][0][m][n] * rstd, u = acc[ai][1][m][n] * rstd;
                float o[4];
#pragma unroll
                for (int j = 0; j < 4; ++j) o[j] = g[j] * __builtin_amdgcn_rcpf(1.0f + __builtin_amdgcn_exp2f(g[j] * -1.4426950408889634f)) * u[j];
                w[n].x = pk_bf16(o[0], o[1]); w[n].y = pk_bf16(o[2], o[3]);
            }
            *(u32x4*)(act + (size_t)row * F_ + f0 + wc * 32 + pcol) = pair_swap(w[0], w[1]);
        }
    WAIT_V(0); STAMP_AT(); STAMP_RT2();
#undef G_SA
#undef G_SB
#undef G_STAGE
#undef G_LDA
#undef G_LDB
#undef G_MMA
#undef G_WAITB
#undef G_FR
}

constexpr int AT_QB = 256, AT_ROWS = AT_QB + 256, AT_NW = AT_QB / 16;
__global__ __launch_bounds__(AT_NW * 64, 4) void attn_kernel(const bf16_t* __restrict__ qkv, bf16_t* __restrict__ o) {
    __shared__ __attribute__((aligned(16))) unsigned char sm[2 * AT_ROWS * 64];
    LAS unsigned char* lk = (LAS unsigned char*)sm; LAS unsigned char* lv = lk + AT_ROWS * 64;
    const int tid = threadIdx.x, wid = __builtin_amdgcn_readfirstlane(tid >> 6), lane = tid & 63, fr = lane & 15, fq = lane >> 4;
    const int head = blockIdx.x & 31, qb = blockIdx.x >> 5, q0 = qb * AT_QB;
    const bf16_t* Q = qkv + (size_t)head * S_ * 32;
    const bf16_t* Kg = qkv + (size_t)(32 + head) * S_ * 32;
    const bf16_t* Vg = qkv + (size_t)(64 + head) * S_ * 32;
#pragma unroll
    for (int pc = wid; pc < AT_ROWS / 16; pc += AT_NW) {
        const int row = pc * 16 + (lane >> 2), slot = lane & 3;
        int key = q0 - 256 + row; key = key < 0 ? 0 : (key > S_ - 1 ? S_ - 1 : key);
        const int chk = slot ^ ((-(row >> 2)) & 3), chv = slot ^ (((row >> 2) & 1) << 1);
        glds16(Kg + (size_t)key * 32 + chk * 8, lk + pc * 1024);
        glds16(Vg + (size_t)key * 32 + chv * 8, lv + pc * 1024);
    }
    const int qrow = q0 + wid * 16 + fr;
    bf16x8 qf;
    asm volatile("global_load_dwordx4 %0, %1, off" : "=v"(qf) : "v"(Q + (size_t)qrow * 32 + fq * 8));
    asm volatile("s_waitcnt vmcnt(0)" : "+v"(qf) :: "memory");
    __builtin_amdgcn_s_barrier();
    const int kbase = q0 + wid * 16 - 256;
    const int istart = kbase < 0 ? (-kbase) >> 4 : 0;
    f32x4 s[17];
    const int koff = (wid * 16 + fr) * 64 + ((fq ^ ((-(fr >> 2)) & 3)) * 16);
#pragma unroll
    for (int i = 0; i < 17; ++i) {
        const bf16x8 kf = *(const LAS bf16x8*)(lk + koff + i * 1024);
        s[i] = __builtin_amdgcn_mfma_f32_16x16x32_bf16(kf, qf, (f32x4){0.f, 0.f, 0.f, 0.f}, 0, 0, 0);
    }
    asm volatile("s_nop 7\n\ts_nop 7\n\ts_nop 3" : "+v"(s[0]), "+v"(s[1]), "+v"(s[2]), "+v"(s[3]), "+v"(s[4]), "+v"(s[5]), "+v"(s[6]), "+v"(s[7]), "+v"(s[8]),
                 "+v"(s[9]), "+v"(s[10]), "+v"(s[11]), "+v"(s[12]), "+v"(s[13]), "+v"(s[14]), "+v"(s[15]), "+v"(s[16]));
    const float NEG = -3.0e38f;
#pragma unroll
    for (int r = 0; r < 4; ++r) { if (!(4 * fq + r > fr)) s[0][r] = NEG; if (!(4 * fq + r <= fr)) s[16][r] = NEG; }
    if (istart > 0) {
#pragma unroll
        for (int i = 0; i < 16; ++i) if (i < istart) s[i] = (f32x4){NEG, NEG, NEG, NEG};
    }
    float mx = NEG;
#pragma unroll
    for (int i = 0; i < 17; ++i) { asm("v_max3_f32 %0, %1, %2, %3" : "=v"(mx) : "v"(mx), "v"(s[i][0]), "v"(s[i][1]));
                                   asm("v_max3_f32 %0, %1, %2, %3" : "=v"(mx) : "v"(mx), "v"(s[i][2]), "v"(s[i][3])); }
    mx = fmaxf(mx, __shfl_xor(mx, 16)); mx = fmaxf(mx, __shfl_xor(mx, 32));
    const float c = 0.17677669529663687f * 1.4426950408889634f;
    const float mc = -mx * c;
    typedef float f32x2 __attribute__((ext_vector_type(2)));
    const f32x2 c2 = {c, c}, mc2 = {mc, mc};
#pragma unroll
    for (int i = 0; i < 17; ++i) {
        const f32x2 lo = __builtin_elementwise_fma((f32x2){s[i][0], s[i][1]}, c2, mc2), hi = __builtin_elementwise_fma((f32x2){s[i][2], s[i][3]}, c2, mc2);
        s[i][0] = __builtin_amdgcn_exp2f(lo.x); s[i][1] = __builtin_amdgcn_exp2f(lo.y); s[i][2] = __builtin_amdgcn_exp2f(hi.x); s[i][3] = __builtin_amdgcn_exp2f(hi.y);
    }
    f32x4 oacc[2] = {(f32x4){0.f, 0.f, 0.f, 0.f}, (f32x4){0.f, 0.f, 0.f, 0.f}};
    f32x4 osum = (f32x4){0.f, 0.f, 0.f, 0.f};
    const bf16x8 ones = (bf16x8){0x3F80, 0x3F80, 0x3F80, 0x3F80, 0x3F80, 0x3F80, 0x3F80, 0x3F80};
    const int q4 = (lane & 15) >> 2, p4 = lane & 3;
    const unsigned va0 = (unsigned)(size_t)lv + (wid * 16 + 4 * fq + q4) * 64 + p4 * 8 + ((fq & 1) << 5), va1 = va0 ^ 32;
    s16x4 vr[2][4];
#define V_READ(SET, cc) do { vr[SET][0] = tr_read_a<(cc) * 2048>(va0); vr[SET][1] = tr_read_a<(cc) * 2048 + ((cc) < 8 ? 1024 : 0)>(va0); \
        vr[SET][2] = tr_read_a<(cc) * 2048>(va1); vr[SET][3] = tr_read_a<(cc) * 2048 + ((cc) < 8 ? 1024 : 0)>(va1); } while (0)
#define V_STEP(cc) do { constexpr int cur = (cc) & 1; \
        if constexpr ((cc) < 8) { V_READ(cur ^ 1, (cc) + 1); asm volatile("s_waitcnt lgkmcnt(4)" : "+v"(vr[cur][0]), "+v"(vr[cur][1]), "+v"(vr[cur][2]), "+v"(vr[cur][3])); } \
        else asm volatile("s_waitcnt lgkmcnt(0)" : "+v"(vr[cur][0]), "+v"(vr[cur][1]), "+v"(vr[cur][2]), "+v"(vr[cur][3])); \
        u32x4 pw; pw.x = pk_bf16(s[2 * (cc)][0], s[2 * (cc)][1]); pw.y = pk_bf16(s[2 * (cc)][2], s[2 * (cc)][3]); \
        if constexpr ((cc) < 8) { pw.z = pk_bf16(s[2 * (cc) + 1][0], s[2 * (cc) + 1][1]); pw.w = pk_bf16(s[2 * (cc) + 1][2], s[2 * (cc) + 1][3]); } else { pw.z = 0u; pw.w = 0u; } \
        const bf16x8 pf = __builtin_bit_cast(bf16x8, pw); \
        osum = __builtin_amdgcn_mfma_f32_16x16x32_bf16(ones, pf, osum, 0, 0, 0); \
        oacc[0] = __builtin_amdgcn_mfma_f32_16x16x32_bf16(__builtin_shufflevector(vr[cur][0], vr[cur][1], 0, 1, 2, 3, 4, 5, 6, 7), pf, oacc[0], 0, 0, 0); \
        oacc[1] = __builtin_amdgcn_mfma_f32_16x16x32_bf16(__builtin_shufflevector(vr[cur][2], vr[cur][3], 0, 1, 2, 3, 4, 5, 6, 7), pf, oacc[1], 0, 0, 0); } while (0)
    V_READ(0, 0);
    V_STEP(0); V_STEP(1); V_STEP(2); V_STEP(3); V_STEP(4); V_STEP(5); V_STEP(6); V_STEP(7); V_STEP(8);
#undef V_READ
#undef V_STEP
    const float inv = 1.0f / osum[0];
#pragma unroll
    for (int dt = 0; dt < 2; ++dt) {
        u32x2 w; w.x = pk_bf16(oacc[dt][0] * inv, oacc[dt][1] * inv); w.y = pk_bf16(oacc[dt][2] * inv, oacc[dt][3] * inv);
        *(u32x2*)(o + (size_t)qrow * D_ + head * 32 + dt * 16 + 4 * fq) = w;
    }
}

extern "C" void kernel_launch(void* const* d_in, const int* in_sizes, int n_in, void* d_out, int out_size, void* d_ws, size_t ws_size,
                              hipStream_t stream) {
    const bf16_t* x = (const bf16_t*)d_in[0];
    const bf16_t* wqkv = (const bf16_t*)d_in[1];
    const bf16_t* wo = (const bf16_t*)d_in[2];
    const bf16_t* w1 = (const bf16_t*)d_in[4];
    const bf16_t* w3 = (const bf16_t*)d_in[5];
    const bf16_t* w2 = (const bf16_t*)d_in[6];
    bf16_t* out = (bf16_t*)d_out;
    char* ws = (char*)d_ws;
    bf16_t* xn = (bf16_t*)(ws);
    bf16_t* qkv = (bf16_t*)(ws + (size_t)4 * 1024 * 1024);
    bf16_t* o = (bf16_t*)(ws + (size_t)20 * 1024 * 1024);
    bf16_t* h = (bf16_t*)(ws + (size_t)24 * 1024 * 1024);
    bf16_t* hn = (bf16_t*)(ws + (size_t)28 * 1024 * 1024);
    bf16_t* act = (bf16_t*)(ws + (size_t)32 * 1024 * 1024);
    float* meanx = (float*)(ws + (size_t)48 * 1024 * 1024);
    bf16_t* wbar = (bf16_t*)(ws + (size_t)48 * 1024 * 1024 + 65536);
    float* sqpart = (float*)(ws + (size_t)49 * 1024 * 1024);

    {
        auto kern = gemm_pp<128, 192, 2, 4, 3, EPI_QKV, 8, 4, 1024>;
        const int shm = 3 * (128 + 192) * 128;
        (void)hipFuncSetAttribute((const void*)kern, hipFuncAttributeMaxDynamicSharedMemorySize, shm);
        kern<<<256, 512, shm, stream>>>(QKV_LNF ? x : xn, D_, wqkv, 3 * D_, wo, qkv, 16, meanx, wbar, nullptr, nullptr);
    }
    attn_kernel<<<32 * (S_ / AT_QB), AT_NW * 64, 0, stream>>>(qkv, o);
    {
        auto kern = gemm_pp<128, 64, 4, 2, 6, EPI_WO, 4, 8, 1024>;
        const int shm = 6 * (128 + 64) * 128 + 2048;
        (void)hipFuncSetAttribute((const void*)kern, hipFuncAttributeMaxDynamicSharedMemorySize, shm);
        kern<<<256, 512, shm, stream>>>(o, D_, wo, D_, x, h, 16, meanx, wbar, hn, sqpart);
    }
    {
        const int shm = 8 * 16384 + 2048;
        (void)hipFuncSetAttribute((const void*)gemm_gateup, hipFuncAttributeMaxDynamicSharedMemorySize, shm);
        gemm_gateup<<<256, 512, shm, stream>>>(hn, w1, w3, act, (const unsigned*)w2, sqpart);
    }
    {
        auto kern = gemm_pp<128, 64, 4, 2, 6, EPI_RES, 4, 8, 3584>;
        const int shm = 6 * (128 + 64) * 128;
        (void)hipFuncSetAttribute((const void*)kern, hipFuncAttributeMaxDynamicSharedMemorySize, shm);
        kern<<<256, 512, shm, stream>>>(act, F_, w2, D_, h, out, 16, nullptr, nullptr, nullptr, nullptr);
    }
}
```

```cpp
#include <hip/hip_runtime.h>
#include <hip/hip_bf16.h>
#include <stdint.h>

typedef unsigned short bf16_t;
typedef short bf16x8 __attribute__((ext_vector_type(8)));
typedef short s16x4 __attribute__((ext_vector_type(4)));
typedef float f32x4 __attribute__((ext_vector_type(4)));
typedef unsigned u32x4 __attribute__((ext_vector_type(4)));
typedef unsigned u32x2 __attribute__((ext_vector_type(2)));
#define LAS __attribute__((address_space(3)))
#define DI __device__ __forceinline__

constexpr int S_ = 2048, D_ = 1024, F_ = 3584, H_ = 32, DH_ = 32, WIN_ = 256;

DI float bf2f(unsigned short b) { return __uint_as_float(((unsigned)b) << 16); }
DI unsigned pk_bf16(float lo, float hi) {
    typedef __bf16 bf2 __attribute__((ext_vector_type(2)));
    bf2 v; v.x = (__bf16)lo; v.y = (__bf16)hi;
    return __builtin_bit_cast(unsigned, v);
}
DI s16x4 tr_read(LAS unsigned char* p) { return __builtin_amdgcn_ds_read_tr16_b64_v4i16((LAS s16x4*)p); }
template <int OFF> DI s16x4 tr_read_a(unsigned addr) { s16x4 r; asm volatile("ds_read_b64_tr_b16 %0, %1 offset:%2" : "=v"(r) : "v"(addr), "i"(OFF)); return r; }
DI void glds16(const void* g, LAS unsigned char* l) {
    __builtin_amdgcn_global_load_lds((const __attribute__((address_space(1))) unsigned*)g, (LAS unsigned*)l, 16, 0, 0);
}
#define WAIT_V(n) asm volatile("s_waitcnt vmcnt(" #n ")" ::: "memory")
#define WAIT_L(n) asm volatile("s_waitcnt lgkmcnt(" #n ")" ::: "memory")
#define BAR __builtin_amdgcn_s_barrier()
#define SCHED __builtin_amdgcn_sched_barrier(0)
#define STAMP_DECL(kid)
#define STAMP_AT()
#define STAMP_RT()
#define STAMP_RT2()

#define CODE_TOUCH(SYM, NB, ACC) do { unsigned lo_, hi_; \
        asm volatile("s_getpc_b64 s[20:21]\n\ts_add_u32 s20, s20, " #SYM "@rel32@lo+4\n\ts_addc_u32 s21, s21, " #SYM "@rel32@hi+12\n\ts_mov_b32 %0, s20\n\ts_mov_b32 %1, s21" \
                     : "=s"(lo_), "=s"(hi_) :: "s20", "s21", "scc"); \
        const unsigned o_ = (threadIdx.x & 63) * 128u; \
        if (o_ < (NB)) ACC += *(const __attribute__((address_space(1))) unsigned*)((((unsigned long long)hi_ << 32) | lo_) + o_); } while (0)
extern "C" __global__ __launch_bounds__(256) void k_ln(const bf16_t* __restrict__ in, bf16_t* __restrict__ out, const unsigned* __restrict__ pf, unsigned pf_lines,
                                                 const unsigned* __restrict__ pfb, unsigned pfb_lines) {
    const int row = blockIdx.x * 4 + (threadIdx.x >> 6), lane = threadIdx.x & 63;
    const u32x4* p = (const u32x4*)(in + (size_t)row * D_);
    u32x4 a = p[lane], b = p[lane + 64];
    unsigned pfv = 0;
    if (blockIdx.x < 8 && threadIdx.x < 64) { CODE_TOUCH(k_gu, 6400, pfv); CODE_TOUCH(k_down, 3328, pfv); }
    { const unsigned li = blockIdx.x * 256u + threadIdx.x; if (li < pf_lines) pfv += pf[(size_t)li * 32];
      else if (li - pf_lines < pfb_lines) pfv += pfb[(size_t)(li - pf_lines) * 32]; }
    float v[16];
#pragma unroll
    for (int i = 0; i < 4; ++i) { v[2 * i] = __uint_as_float(a[i] << 16); v[2 * i + 1] = __uint_as_float(a[i] & 0xffff0000u);
                                  v[8 + 2 * i] = __uint_as_float(b[i] << 16); v[8 + 2 * i + 1] = __uint_as_float(b[i] & 0xffff0000u); }
    float s = 0.f;
#pragma unroll
    for (int i = 0; i < 16; ++i) s += v[i];
#pragma unroll
    for (int o = 32; o >= 1; o >>= 1) s += __shfl_xor(s, o);
    const float mean = s * (1.0f / D_);
    float q = 0.f;
#pragma unroll
    for (int i = 0; i < 16; ++i) { v[i] -= mean; q += v[i] * v[i]; }
#pragma unroll
    for (int o = 32; o >= 1; o >>= 1) q += __shfl_xor(q, o);
    const float rstd = 1.0f / sqrtf(q * (1.0f / D_) + 1e-5f);
    u32x4 oa, ob;
#pragma unroll
    for (int i = 0; i < 4; ++i) { oa[i] = pk_bf16(v[2 * i] * rstd, v[2 * i + 1] * rstd); ob[i] = pk_bf16(v[8 + 2 * i] * rstd, v[8 + 2 * i + 1] * rstd); }
    u32x4* po = (u32x4*)(out + (size_t)row * D_);
    po[lane] = oa; po[lane + 64] = ob;
    asm volatile("" :: "v"(pfv));
}

DI int bx_of_k(int k) { return ((k >> 1) & 1) | (((k >> 3) & 1) << 1); }

enum { EPI_QKV = 0, EPI_RES = 1 };
#define QKV_LNF 1
DI u32x4 pair_swap(u32x2 t0, u32x2 t1) {
    auto a = __builtin_amdgcn_permlane16_swap(t0.x, t1.x, false, false);
    auto b = __builtin_amdgcn_permlane16_swap(t0.y, t1.y, false, false);
    return (u32x4){a[0], b[0], a[1], b[1]};
}

template <int N> DI void wait_v() { asm volatile("s_waitcnt vmcnt(%0)" :: "i"(N) : "memory"); }
template <int OFF> DI bf16x8 ld128_a(unsigned addr) { bf16x8 r; asm volatile("ds_read_b128 %0, %1 offset:%2" : "=v"(r) : "v"(addr), "i"(OFF)); return r; }
typedef __bf16 bf2_t __attribute__((ext_vector_type(2)));
DI float dot2_one(unsigned w, float c) { return __builtin_amdgcn_fdot2_f32_bf16(__builtin_bit_cast(bf2_t, w), __builtin_bit_cast(bf2_t, 0x3F803F80u), c, false); }
DI float dot2_sq(unsigned w, float c) { return __builtin_amdgcn_fdot2_f32_bf16(__builtin_bit_cast(bf2_t, w), __builtin_bit_cast(bf2_t, w), c, false); }
DI void row_stats_acc(const bf16x8& f, float& rs, float& rq) { const u32x4 w = __builtin_bit_cast(u32x4, f);
#pragma unroll
    for (int i = 0; i < 4; ++i) { rs = dot2_one(w[i], rs); rq = dot2_sq(w[i], rq); } }
DI void col_sum_acc(const s16x4& lo, const s16x4& hi, float& cs) { const u32x2 a = __builtin_bit_cast(u32x2, lo), b = __builtin_bit_cast(u32x2, hi);
    cs = dot2_one(a.x, cs); cs = dot2_one(a.y, cs); cs = dot2_one(b.x, cs); cs = dot2_one(b.y, cs); }
DI float red_fq(float v) { v += __shfl_xor(v, 16); v += __shfl_xor(v, 32); return v; }

template <int BM, int BN, int WM, int WN, int NBUF, int EPI, int RM, int RN, int K>
DI void gemm_pp(const bf16_t* __restrict__ A, int lda, const bf16_t* __restrict__ W, int ldb,
                const bf16_t* __restrict__ res, bf16_t* __restrict__ out, int tiles_m) {
    extern __shared__ __attribute__((aligned(16))) unsigned char smem_raw[];
    LAS unsigned char* lds = (LAS unsigned char*)smem_raw;
    constexpr int MT = BM / WM / 16, NT = BN / WN / 16;
    constexpr int NPA = BM / 64, NPB = BN / 64, NP = NPA + NPB;
    constexpr int ABYTES = BM * 128, TILE_BYTES = (BM + BN) * 128;
    constexpr int nt = K / 64;
    constexpr bool LNF = (EPI == EPI_QKV) && QKV_LNF;
    static_assert(WM * WN == 8, "8 waves");
    static_assert(MT == 2 || MT == 4, "MT");
    static_assert(!LNF || (MT == 4 && WN == 4 && WM == 2), "LN fold geometry");
    STAMP_DECL(EPI == EPI_QKV ? 0 : (K == 1024 ? 1 : 2)); STAMP_RT(); STAMP_AT();
    const int tid = threadIdx.x, wid = __builtin_amdgcn_readfirstlane(tid >> 6), lane = tid & 63;
    const int wm = wid / WN, wn = wid % WN, fr = lane & 15, fq = lane >> 4;
    int tm, tn;
    { const int bid = blockIdx.x, xcd = bid & 7, idx = bid >> 3; const int regm = tiles_m / RM;
      const int rr = xcd % regm, rc = xcd / regm; tm = rr * RM + (idx % RM); tn = rc * RN + (idx / RM); }
    const int m0 = tm * BM, n0 = tn * BN;
    const bf16_t* asrc[NPA]; const bf16_t* bsrc[NPB];
#pragma unroll
    for (int i = 0; i < NPA; ++i) { const int row = (wid + 8 * i) * 8 + (lane >> 3), ch = (lane & 7) ^ ((row >> 1) & 7);
        asrc[i] = A + (size_t)(m0 + row) * lda + ch * 8; }
#pragma unroll
    for (int j = 0; j < NPB; ++j) { const int k = wid * 8 + (lane >> 3), s16 = lane & 7, n16 = (s16 >> 1) ^ bx_of_k(k);
        bsrc[j] = W + (size_t)k * ldb + n0 + j * 64 + n16 * 16 + (s16 & 1) * 8; }
    const size_t bstep = (size_t)64 * ldb;
    auto stage = [&](int t, int buf) {
        LAS unsigned char* base = lds + buf * TILE_BYTES + wid * 1024;
#pragma unroll
        for (int i = 0; i < NPA; ++i) glds16(asrc[i] + (size_t)t * 64, base + i * 8192);
#pragma unroll
        for (int j = 0; j < NPB; ++j) glds16(bsrc[j] + (size_t)t * bstep, base + ABYTES + j * 8192);
    };
    const int rotm = LNF ? wn : 0, krot = LNF ? wm : 0;
    const int sw = (fr >> 1) & 7;
    unsigned aadr[MT][2], badr[NT][2];
#pragma unroll
    for (int j = 0; j < MT; ++j)
#pragma unroll
        for (int sl = 0; sl < 2; ++sl) aadr[j][sl] = (unsigned)(size_t)lds + (wm * MT * 16 + ((j + rotm) & (MT - 1)) * 16 + fr) * 128 + (((((sl ^ krot) * 4) + fq) ^ sw) * 16);
    { const int q = (lane & 15) >> 2, p = lane & 3, xl = ((q >> 1) & 1) | ((fq & 1) << 1);
#pragma unroll
      for (int n = 0; n < NT; ++n) { const int ntg = wn * NT + n;
          const unsigned b0 = (unsigned)(size_t)lds + ABYTES + (ntg >> 2) * 8192 + (8 * fq + q) * 128 + (((ntg & 3) ^ xl) * 32) + p * 8;
          badr[n][0] = b0 + krot * 4096; badr[n][1] = b0 + (1 - krot) * 4096; } }
    f32x4 acc[MT][NT];
#pragma unroll
    for (int m = 0; m < MT; ++m)
#pragma unroll
        for (int n = 0; n < NT; ++n) acc[m][n] = (f32x4){0.f, 0.f, 0.f, 0.f};
    f32x4 rsacc = (f32x4){0.f, 0.f, 0.f, 0.f}, rqacc = rsacc, csacc[NT];
#pragma unroll
    for (int n = 0; n < NT; ++n) csacc[n] = rsacc;
    const bf16x8 ones = (bf16x8){0x3F80, 0x3F80, 0x3F80, 0x3F80, 0x3F80, 0x3F80, 0x3F80, 0x3F80};
    bf16x8 afA[MT][2], afB[MT][2]; s16x4 bA[NT][4], bB[NT][4];
#define P_READ(AF, BR, buf) do { const unsigned bo_ = (unsigned)((buf) * TILE_BYTES); \
        if constexpr (LNF) { \
            _Pragma("unroll") for (int n = 0; n < NT; ++n) { const unsigned b0_ = badr[n][0] + bo_, b1_ = badr[n][1] + bo_; \
                BR[n][0] = tr_read_a<0>(b0_); BR[n][1] = tr_read_a<512>(b0_); BR[n][2] = tr_read_a<0>(b1_); BR[n][3] = tr_read_a<512>(b1_); } \
            _Pragma("unroll") for (int j = 0; j < MT; ++j) { AF[j][0] = ld128_a<0>(aadr[j][0] + bo_); AF[j][1] = ld128_a<0>(aadr[j][1] + bo_); } \
        } else { \
            _Pragma("unroll") for (int n = 0; n < NT; ++n) { const unsigned ba = badr[n][0] + bo_; \
                BR[n][0] = tr_read_a<0>(ba); BR[n][1] = tr_read_a<512>(ba); BR[n][2] = tr_read_a<4096>(ba); BR[n][3] = tr_read_a<4608>(ba); } \
            const unsigned a0_ = aadr[0][0] + bo_, a1_ = aadr[0][1] + bo_; \
            AF[0][0] = ld128_a<0>(a0_); AF[0][1] = ld128_a<0>(a1_); AF[1][0] = ld128_a<2048>(a0_); AF[1][1] = ld128_a<2048>(a1_); \
            if constexpr (MT == 4) { AF[MT - 2][0] = ld128_a<4096>(a0_); AF[MT - 2][1] = ld128_a<4096>(a1_); AF[MT - 1][0] = ld128_a<6144>(a0_); AF[MT - 1][1] = ld128_a<6144>(a1_); } } } while (0)
#define P_WAITL(AF, BR) do { _Pragma("unroll") for (int n = 0; n < NT; ++n) \
        asm volatile("s_waitcnt lgkmcnt(0)" : "+v"(BR[n][0]), "+v"(BR[n][1]), "+v"(BR[n][2]), "+v"(BR[n][3]) :: "memory"); \
        _Pragma("unroll") for (int m = 0; m < MT; ++m) asm volatile("" : "+v"(AF[m][0]), "+v"(AF[m][1])); } while (0)
#define P_MMA(AF, BR) do { _Pragma("unroll") for (int kk = 0; kk < 2; ++kk) _Pragma("unroll") for (int m = 0; m < MT; ++m) _Pragma("unroll") for (int n = 0; n < NT; ++n) \
        acc[m][n] = __builtin_amdgcn_mfma_f32_16x16x32_bf16(__builtin_shufflevector(BR[n][2 * kk], BR[n][2 * kk + 1], 0, 1, 2, 3, 4, 5, 6, 7), AF[m][kk], acc[m][n], 0, 0, 0); \
        if constexpr (LNF) { _Pragma("unroll") for (int kk = 0; kk < 2; ++kk) { rsacc = __builtin_amdgcn_mfma_f32_16x16x32_bf16(ones, AF[0][kk], rsacc, 0, 0, 0); \
                rqacc = __builtin_amdgcn_mfma_f32_16x16x32_bf16(AF[0][kk], AF[0][kk], rqacc, 0, 0, 0); } \
            _Pragma("unroll") for (int n = 0; n < NT; ++n) csacc[n] = __builtin_amdgcn_mfma_f32_16x16x32_bf16(__builtin_shufflevector(BR[n][0], BR[n][1], 0, 1, 2, 3, 4, 5, 6, 7), ones, csacc[n], 0, 0, 0); } } while (0)
#define P_STEP(AFC, BRC, AFN, BRN, FULL, WN_, RESLD) do { \
        P_WAITL(AFC, BRC); \
        wait_v<WN_>(); \
        BAR; \
        const int nb_ = (buf + 1 == NBUF) ? 0 : buf + 1; \
        if (FULL || t + 1 < nt) P_READ(AFN, BRN, nb_); \
        if (FULL || t + NBUF < nt) stage(t + NBUF, buf); \
        if (RESLD) { _Pragma("unroll") for (int m = 0; m < MT; ++m) _Pragma("unroll") for (int n = 0; n < NT; ++n) \
            rres[m][n] = *(const u32x2*)(res + (size_t)(m0 + (wm * MT + m) * 16 + fr) * D_ + n0 + (wn * NT + n) * 16 + 4 * fq); } \
        P_MMA(AFC, BRC); \
        buf = nb_; ++t; } while (0)
    u32x2 rres[MT][NT];
    constexpr int NR = (EPI == EPI_RES) ? MT * NT : 0;
#pragma unroll
    for (int t = 0; t < NBUF; ++t) stage(t, t);
    wait_v<(NBUF - 1) * NP>();
    BAR;
    STAMP_AT();
    P_READ(afA, bA, 0);
    int buf = 0, t = 0;
    constexpr int nmain = ((nt - NBUF) / 2) * 2;
    for (; t < nmain;) {
        P_STEP(afA, bA, afB, bB, true, (NBUF - 2) * NP, false);
        P_STEP(afB, bB, afA, bA, true, (NBUF - 2) * NP, false);
    }
#define P_TAILW(tt) ((((tt) + NBUF - 1 < nt - 1 ? (tt) + NBUF - 1 : nt - 1) - ((tt) + 1) > 0 ? (((tt) + NBUF - 1 < nt - 1 ? (tt) + NBUF - 1 : nt - 1) - ((tt) + 1)) : 0) * NP + ((tt) > nt - NBUF ? NR : 0))
#define P_TAIL2(tt) do { t = (tt); P_STEP(afA, bA, afB, bB, false, P_TAILW(tt), (NR > 0 && (tt) == nt - NBUF)); \
        P_STEP(afB, bB, afA, bA, false, P_TAILW((tt) + 1), (NR > 0 && (tt) + 1 == nt - NBUF)); } while (0)
    if constexpr (nmain + 0 < nt) P_TAIL2(nmain + 0);
    if constexpr (nmain + 2 < nt) P_TAIL2(nmain + 2);
    if constexpr (nmain + 4 < nt) P_TAIL2(nmain + 4);
    if constexpr (nmain + 6 < nt) P_TAIL2(nmain + 6);
    static_assert(nmain + 8 >= nt, "tail");
#undef P_TAILW
#undef P_TAIL2
    STAMP_AT();
#undef P_READ
#undef P_WAITL
#undef P_MMA
#undef P_STEP
    if constexpr (EPI == EPI_RES) {
#pragma unroll
        for (int m = 0; m < MT; ++m)
#pragma unroll
            for (int n = 0; n < NT; ++n) asm volatile("" : "+v"(rres[m][n]));
    }
    typedef float f32x2 __attribute__((ext_vector_type(2)));
    LAS f32x2* stat = (LAS f32x2*)lds;
    LAS float* cpart = (LAS float*)(lds + BM * 8);
    if constexpr (LNF) {
        const float rq = (fr & 2) ? ((fr & 1) ? rqacc[3] : rqacc[2]) : ((fr & 1) ? rqacc[1] : rqacc[0]);
        const float mean = rsacc[0] * (1.0f / K), var = fmaxf(rq * (1.0f / K) - mean * mean, 0.f);
        if (fq == (fr >> 2)) stat[wm * 64 + wn * 16 + fr] = (f32x2){mean, 1.0f / sqrtf(var + 1e-5f)};
#pragma unroll
        for (int n = 0; n < NT; ++n) if (fr == 0) *(LAS f32x4*)(cpart + wm * BN + (wn * NT + n) * 16 + 4 * fq) = csacc[n];
        __syncthreads();
    }
    unsigned pfv = 0;
    if constexpr (EPI == EPI_QKV) { const unsigned li = blockIdx.x * 512u + threadIdx.x; if (li < (unsigned)((size_t)D_ * D_ * 2 / 128)) pfv = ((const unsigned*)res)[(size_t)li * 32];
        if (blockIdx.x >= 248 && wid == 0) { CODE_TOUCH(k_attn, 3456, pfv); CODE_TOUCH(k_wo, 4992, pfv); } }
    const int pcol = (fq & 1) ? 16 + 4 * (fq - 1) : 4 * fq;
    f32x4 csum[NT];
    if constexpr (LNF) {
#pragma unroll
        for (int n = 0; n < NT; ++n) csum[n] = *(const LAS f32x4*)(cpart + (wn * NT + n) * 16 + 4 * fq) + *(const LAS f32x4*)(cpart + BN + (wn * NT + n) * 16 + 4 * fq);
    }
#pragma unroll
    for (int m = 0; m < MT; ++m) {
        const int lrow = (wm * MT + ((m + rotm) & (MT - 1))) * 16 + fr, row = m0 + lrow;
        f32x2 st; if constexpr (LNF) st = stat[lrow];
        u32x2 w[NT];
#pragma unroll
        for (int n = 0; n < NT; ++n) {
            f32x4 v = acc[m][n];
            if constexpr (LNF) v = (v - csum[n] * st.x) * st.y;
            if constexpr (EPI == EPI_RES) { const u32x2 r = rres[m][n];
                v[0] += __uint_as_float(r.x << 16); v[1] += __uint_as_float(r.x & 0xffff0000u);
                v[2] += __uint_as_float(r.y << 16); v[3] += __uint_as_float(r.y & 0xffff0000u); }
            w[n].x = pk_bf16(v[0], v[1]); w[n].y = pk_bf16(v[2], v[3]);
        }
#pragma unroll
        for (int n = 0; n + 1 < NT; n += 2) {
            const u32x4 o = pair_swap(w[n], w[n + 1]);
            const int col = n0 + (wn * NT + n) * 16 + pcol;
            if constexpr (EPI == EPI_QKV) *(u32x4*)(out + ((size_t)(col >> 5) * S_ + row) * 32 + (col & 31)) = o;
            else *(u32x4*)(out + (size_t)row * D_ + col) = o;
        }
        if constexpr (NT & 1) {
            const int col = n0 + (wn * NT + NT - 1) * 16 + 4 * fq;
            if constexpr (EPI == EPI_QKV) *(u32x2*)(out + ((size_t)(col >> 5) * S_ + row) * 32 + (col & 31)) = w[NT - 1];
            else *(u32x2*)(out + (size_t)row * D_ + col) = w[NT - 1];
        }
    }
    asm volatile("" :: "v"(pfv));
    WAIT_V(0); STAMP_AT(); STAMP_RT2();
}

extern "C" __global__ __launch_bounds__(512, 2) void k_qkv(const bf16_t* __restrict__ A, const bf16_t* __restrict__ W, const bf16_t* __restrict__ wo, bf16_t* __restrict__ out) {
    gemm_pp<128, 192, 2, 4, 3, EPI_QKV, 8, 4, 1024>(A, D_, W, 3 * D_, wo, out, 16);
}
extern "C" __global__ __launch_bounds__(512, 2) void k_wo(const bf16_t* __restrict__ A, const bf16_t* __restrict__ W, const bf16_t* __restrict__ res, bf16_t* __restrict__ out) {
    gemm_pp<128, 64, 4, 2, 6, EPI_RES, 4, 8, 1024>(A, D_, W, D_, res, out, 16);
}
extern "C" __global__ __launch_bounds__(512, 2) void k_down(const bf16_t* __restrict__ A, const bf16_t* __restrict__ W, const bf16_t* __restrict__ res, bf16_t* __restrict__ out) {
    gemm_pp<128, 64, 4, 2, 6, EPI_RES, 4, 8, 3584>(A, F_, W, D_, res, out, 16);
}

extern "C" __global__ __launch_bounds__(512, 2) void k_gu(const bf16_t* __restrict__ A, const bf16_t* __restrict__ W1, const bf16_t* __restrict__ W3,
                                                       bf16_t* __restrict__ act, const unsigned* __restrict__ pf) {
    extern __shared__ __attribute__((aligned(16))) unsigned char smem_raw[];
    LAS unsigned char* lds = (LAS unsigned char*)smem_raw;
    constexpr int K = D_, HTB = 16384;
    STAMP_DECL(3); STAMP_RT(); STAMP_AT();
    const int tid = threadIdx.x, wid = __builtin_amdgcn_readfirstlane(tid >> 6), lane = tid & 63;
    const int wr = wid >> 2, wc = wid & 3, fr = lane & 15, fq = lane >> 4;
    if (blockIdx.x >= 224) {
        constexpr unsigned NL = (unsigned)((size_t)F_ * D_ * 2 / 128);
        const unsigned l0 = (blockIdx.x - 224) * 512u + threadIdx.x;
        unsigned a = 0;
#pragma unroll
        for (int i = 0; i < 4; ++i) { const unsigned li = l0 + i * 16384u; if (li < NL) a += ((const unsigned*)W1)[(size_t)li * 32] + ((const unsigned*)W3)[(size_t)li * 32]; }
#pragma unroll
        for (int i = 0; i < 4; ++i) { const unsigned li = l0 + i * 16384u; if (li < NL) a += pf[(size_t)li * 32]; }
        if (threadIdx.x < 64) CODE_TOUCH(k_down, 3328, a);
        asm volatile("" :: "v"(a));
        return;
    }
    int tm, tn;
    { const int bid = blockIdx.x, xcd = bid & 7, idx = bid >> 3; tm = (xcd & 1) * 4 + (idx & 3); tn = (xcd >> 1) * 7 + (idx >> 2); }
    const int m0 = tm * 256, f0 = tn * 128;
    constexpr int nt = K / 64;
    unsigned aoffs[2], boffs[2];
#pragma unroll
    for (int i = 0; i < 2; ++i) { const int row = (wid + 8 * i) * 8 + (lane >> 3), ch = (lane & 7) ^ ((row >> 1) & 7);
        aoffs[i] = (unsigned)(row * K + ch * 8) * 2u;
        const int k = wid * 8 + (lane >> 3), s16 = lane & 7, n16 = (s16 >> 1) ^ bx_of_k(k);
        boffs[i] = (unsigned)(k * F_ + i * 64 + n16 * 16 + (s16 & 1) * 8) * 2u; }
    const char* gA0 = (const char*)(A + (size_t)m0 * K); const char* gA1 = gA0 + (size_t)128 * K * 2;
    const char* gB0 = (const char*)(W1 + f0); const char* gB1 = (const char*)(W3 + f0);
    constexpr size_t akstep = 128, bkstep = (size_t)64 * F_ * 2;
    const unsigned ldsw = (unsigned)wid * 1024u;
#define G_SA(b, h) (((b) * 2 + (h)) * HTB)
#define G_SB(b, h) ((4 + (b) * 2 + (h)) * HTB)
#define G_STAGE(bufoff, gbase, voff) do { _Pragma("unroll") for (int _i = 0; _i < 2; ++_i) glds16((gbase) + (voff)[_i], lds + (bufoff) + ldsw + _i * 8192); } while (0)
    const int sw = (fr >> 1) & 7;
    const int aoff0 = (wr * 64 + fr) * 128 + (((wr * 4 + fq) ^ sw) * 16), aoff1 = aoff0 ^ 64;
    unsigned badr[2][2];
    { const int q = (lane & 15) >> 2, p = lane & 3, xl = ((q >> 1) & 1) | ((fq & 1) << 1);
#pragma unroll
      for (int n = 0; n < 2; ++n) { const int ntg = wc * 2 + n;
          const unsigned b0 = (unsigned)(size_t)lds + 4 * HTB + (ntg >> 2) * 8192 + (8 * fq + q) * 128 + (((ntg & 3) ^ xl) * 32) + p * 8;
          badr[n][0] = b0 + wr * 4096; badr[n][1] = b0 + (1 - wr) * 4096; } }
#define G_LDA(dst, b, h) do { _Pragma("unroll") for (int m = 0; m < 4; ++m) { dst[m][0] = *(const LAS bf16x8*)(lds + G_SA(b, h) + aoff0 + m * 2048); \
        dst[m][1] = *(const LAS bf16x8*)(lds + G_SA(b, h) + aoff1 + m * 2048); } } while (0)
#define G_LDB(dst, bo, h) do { _Pragma("unroll") for (int n = 0; n < 2; ++n) { const unsigned b0_ = badr[n][0] + (bo), b1_ = badr[n][1] + (bo); \
        dst[n][0] = tr_read_a<(h) * HTB>(b0_); dst[n][1] = tr_read_a<(h) * HTB + 512>(b0_); \
        dst[n][2] = tr_read_a<(h) * HTB>(b1_); dst[n][3] = tr_read_a<(h) * HTB + 512>(b1_); } } while (0)
#define G_WAITB(R) do { asm volatile("s_waitcnt lgkmcnt(0)" : "+v"(R[0][0]), "+v"(R[0][1]), "+v"(R[0][2]), "+v"(R[0][3]), "+v"(R[1][0]), "+v"(R[1][1]), "+v"(R[1][2]), "+v"(R[1][3]) :: "memory"); } while (0)
#define G_FR(R, n, k) __builtin_shufflevector(R[n][2 * (k)], R[n][2 * (k) + 1], 0, 1, 2, 3, 4, 5, 6, 7)
#define G_MMA(ai, bj, At, Bt) do { __builtin_amdgcn_s_setprio(1); _Pragma("unroll") for (int m = 0; m < 4; ++m) _Pragma("unroll") for (int n = 0; n < 2; ++n) \
        _Pragma("unroll") for (int k = 0; k < 2; ++k) acc[ai][bj][m][n] = __builtin_amdgcn_mfma_f32_16x16x32_bf16(G_FR(Bt, n, k), At[m][k], acc[ai][bj][m][n], 0, 0, 0); \
        __builtin_amdgcn_s_setprio(0); } while (0)
    f32x4 acc[2][2][4][2];
#pragma unroll
    for (int a = 0; a < 2; ++a)
#pragma unroll
        for (int b = 0; b < 2; ++b)
#pragma unroll
            for (int m = 0; m < 4; ++m)
#pragma unroll
                for (int n = 0; n < 2; ++n) acc[a][b][m][n] = (f32x4){0.f, 0.f, 0.f, 0.f};
    bf16x8 At[4][2]; s16x4 B0[2][4], B1[2][4];
    G_STAGE(G_SB(0, 0), gB0, boffs); G_STAGE(G_SA(0, 0), gA0, aoffs); G_STAGE(G_SB(0, 1), gB1, boffs); G_STAGE(G_SA(0, 1), gA1, aoffs);
    if (wr == 1) BAR;
    WAIT_V(4); BAR;
    G_STAGE(G_SB(1, 0), gB0 + bkstep, boffs); G_STAGE(G_SA(1, 0), gA0 + akstep, aoffs); G_STAGE(G_SB(1, 1), gB1 + bkstep, boffs);
    WAIT_V(6); BAR;
    STAMP_AT();
    for (int t = 0; t < nt; t += 2) {
        const int t1 = t + 1, t2 = (t + 2 < nt) ? t + 2 : nt - 1, t3 = (t + 3 < nt) ? t + 3 : nt - 1;
        G_LDB(B0, 0, 0); SCHED; G_LDA(At, 0, 0); G_STAGE(G_SA(1, 1), gA1 + t1 * akstep, aoffs);
        WAIT_L(8); BAR; G_WAITB(B0); G_MMA(0, 0, At, B0); BAR; SCHED;
        G_LDB(B1, 0, 1); G_STAGE(G_SB(0, 0), gB0 + t2 * bkstep, boffs);
        BAR; G_WAITB(B1); G_MMA(0, 1, At, B1); BAR;
        G_LDA(At, 0, 1); G_STAGE(G_SA(0, 0), gA0 + t2 * akstep, aoffs);
        BAR; WAIT_L(0); G_MMA(1, 0, At, B0); BAR; SCHED;
        G_STAGE(G_SB(0, 1), gB1 + t2 * bkstep, boffs);
        WAIT_V(6); BAR; G_MMA(1, 1, At, B1); BAR;
        G_LDB(B0, 2 * HTB, 0); SCHED; G_LDA(At, 1, 0); G_STAGE(G_SA(0, 1), gA1 + t2 * akstep, aoffs);
        WAIT_L(8); BAR; G_WAITB(B0); G_MMA(0, 0, At, B0); BAR; SCHED;
        G_LDB(B1, 2 * HTB, 1); G_STAGE(G_SB(1, 0), gB0 + t3 * bkstep, boffs);
        BAR; G_WAITB(B1); G_MMA(0, 1, At, B1); BAR;
        G_LDA(At, 1, 1); G_STAGE(G_SA(1, 0), gA0 + t3 * akstep, aoffs);
        BAR; WAIT_L(0); G_MMA(1, 0, At, B0); BAR; SCHED;
        G_STAGE(G_SB(1, 1), gB1 + t3 * bkstep, boffs);
        WAIT_V(6); BAR; G_MMA(1, 1, At, B1); BAR;
    }
    STAMP_AT();
    WAIT_V(0);
    if (wr == 0) BAR;
    const int pcol = (fq & 1) ? 16 + 4 * (fq - 1) : 4 * fq;
#pragma unroll
    for (int ai = 0; ai < 2; ++ai)
#pragma unroll
        for (int m = 0; m < 4; ++m) {
            const int lrow = ai * 128 + wr * 64 + m * 16 + fr, row = m0 + lrow;
            u32x2 w[2];
#pragma unroll
            for (int n = 0; n < 2; ++n) {
                const f32x4 g = acc[ai][0][m][n], u = acc[ai][1][m][n];
                float o[4];
#pragma unroll
                for (int j = 0; j < 4; ++j) o[j] = g[j] * __builtin_amdgcn_rcpf(1.0f + __builtin_amdgcn_exp2f(g[j] * -1.4426950408889634f)) * u[j];
                w[n].x = pk_bf16(o[0], o[1]); w[n].y = pk_bf16(o[2], o[3]);
            }
            *(u32x4*)(act + (size_t)row * F_ + f0 + wc * 32 + pcol) = pair_swap(w[0], w[1]);
        }
    WAIT_V(0); STAMP_AT(); STAMP_RT2();
#undef G_SA
#undef G_SB
#undef G_STAGE
#undef G_LDA
#undef G_LDB
#undef G_MMA
#undef G_WAITB
#undef G_FR
}

constexpr int AT_QB = 256, AT_ROWS = AT_QB + 256, AT_NW = AT_QB / 16;
extern "C" __global__ __launch_bounds__(AT_NW * 64, 4) void k_attn(const bf16_t* __restrict__ qkv, bf16_t* __restrict__ o) {
    __shared__ __attribute__((aligned(16))) unsigned char sm[2 * AT_ROWS * 64];
    LAS unsigned char* lk = (LAS unsigned char*)sm; LAS unsigned char* lv = lk + AT_ROWS * 64;
    const int tid = threadIdx.x, wid = __builtin_amdgcn_readfirstlane(tid >> 6), lane = tid & 63, fr = lane & 15, fq = lane >> 4;
    const int head = blockIdx.x & 31, qb = blockIdx.x >> 5, q0 = qb * AT_QB;
    const bf16_t* Q = qkv + (size_t)head * S_ * 32;
    const bf16_t* Kg = qkv + (size_t)(32 + head) * S_ * 32;
    const bf16_t* Vg = qkv + (size_t)(64 + head) * S_ * 32;
#pragma unroll
    for (int pc = wid; pc < AT_ROWS / 16; pc += AT_NW) {
        const int row = pc * 16 + (lane >> 2), slot = lane & 3;
        int key = q0 - 256 + row; key = key < 0 ? 0 : (key > S_ - 1 ? S_ - 1 : key);
        const int chk = slot ^ ((-(row >> 2)) & 3), chv = slot ^ (((row >> 2) & 1) << 1);
        glds16(Kg + (size_t)key * 32 + chk * 8, lk + pc * 1024);
        glds16(Vg + (size_t)key * 32 + chv * 8, lv + pc * 1024);
    }
    const int qrow = q0 + wid * 16 + fr;
    bf16x8 qf;
    asm volatile("global_load_dwordx4 %0, %1, off" : "=v"(qf) : "v"(Q + (size_t)qrow * 32 + fq * 8));
    asm volatile("s_waitcnt vmcnt(0)" : "+v"(qf) :: "memory");
    __builtin_amdgcn_s_barrier();
    unsigned ctv = 0;
    if (blockIdx.x < 8 && wid == 0) { CODE_TOUCH(k_wo, 4992, ctv); CODE_TOUCH(k_ln, 1152, ctv); CODE_TOUCH(k_gu, 6400, ctv); }
    const int kbase = q0 + wid * 16 - 256;
    const int istart = kbase < 0 ? (-kbase) >> 4 : 0;
    f32x4 s[17];
    const int koff = (wid * 16 + fr) * 64 + ((fq ^ ((-(fr >> 2)) & 3)) * 16);
#pragma unroll
    for (int i = 0; i < 17; ++i) {
        const bf16x8 kf = *(const LAS bf16x8*)(lk + koff + i * 1024);
        s[i] = __builtin_amdgcn_mfma_f32_16x16x32_bf16(kf, qf, (f32x4){0.f, 0.f, 0.f, 0.f}, 0, 0, 0);
    }
    asm volatile("s_nop 7\n\ts_nop 7\n\ts_nop 3" : "+v"(s[0]), "+v"(s[1]), "+v"(s[2]), "+v"(s[3]), "+v"(s[4]), "+v"(s[5]), "+v"(s[6]), "+v"(s[7]), "+v"(s[8]),
                 "+v"(s[9]), "+v"(s[10]), "+v"(s[11]), "+v"(s[12]), "+v"(s[13]), "+v"(s[14]), "+v"(s[15]), "+v"(s[16]));
    const float NEG = -3.0e38f;
#pragma unroll
    for (int r = 0; r < 4; ++r) { if (!(4 * fq + r > fr)) s[0][r] = NEG; if (!(4 * fq + r <= fr)) s[16][r] = NEG; }
    if (istart > 0) {
#pragma unroll
        for (int i = 0; i < 16; ++i) if (i < istart) s[i] = (f32x4){NEG, NEG, NEG, NEG};
    }
    float mx = NEG;
#pragma unroll
    for (int i = 0; i < 17; ++i) { asm("v_max3_f32 %0, %1, %2, %3" : "=v"(mx) : "v"(mx), "v"(s[i][0]), "v"(s[i][1]));
                                   asm("v_max3_f32 %0, %1, %2, %3" : "=v"(mx) : "v"(mx), "v"(s[i][2]), "v"(s[i][3])); }
    mx = fmaxf(mx, __shfl_xor(mx, 16)); mx = fmaxf(mx, __shfl_xor(mx, 32));
    const float c = 0.17677669529663687f * 1.4426950408889634f;
    const float mc = -mx * c;
    typedef float f32x2 __attribute__((ext_vector_type(2)));
    const f32x2 c2 = {c, c}, mc2 = {mc, mc};
#pragma unroll
    for (int i = 0; i < 17; ++i) {
        const f32x2 lo = __builtin_elementwise_fma((f32x2){s[i][0], s[i][1]}, c2, mc2), hi = __builtin_elementwise_fma((f32x2){s[i][2], s[i][3]}, c2, mc2);
        s[i][0] = __builtin_amdgcn_exp2f(lo.x); s[i][1] = __builtin_amdgcn_exp2f(lo.y); s[i][2] = __builtin_amdgcn_exp2f(hi.x); s[i][3] = __builtin_amdgcn_exp2f(hi.y);
    }
    f32x4 oacc[2] = {(f32x4){0.f, 0.f, 0.f, 0.f}, (f32x4){0.f, 0.f, 0.f, 0.f}};
    f32x4 osum = (f32x4){0.f, 0.f, 0.f, 0.f};
    const bf16x8 ones = (bf16x8){0x3F80, 0x3F80, 0x3F80, 0x3F80, 0x3F80, 0x3F80, 0x3F80, 0x3F80};
    const int q4 = (lane & 15) >> 2, p4 = lane & 3;
    const unsigned va0 = (unsigned)(size_t)lv + (wid * 16 + 4 * fq + q4) * 64 + p4 * 8 + ((fq & 1) << 5), va1 = va0 ^ 32;
    s16x4 vr[2][4];
#define V_READ(SET, cc) do { vr[SET][0] = tr_read_a<(cc) * 2048>(va0); vr[SET][1] = tr_read_a<(cc) * 2048 + ((cc) < 8 ? 1024 : 0)>(va0); \
        vr[SET][2] = tr_read_a<(cc) * 2048>(va1); vr[SET][3] = tr_read_a<(cc) * 2048 + ((cc) < 8 ? 1024 : 0)>(va1); } while (0)
#define V_STEP(cc) do { constexpr int cur = (cc) & 1; \
        if constexpr ((cc) < 8) { V_READ(cur ^ 1, (cc) + 1); asm volatile("s_waitcnt lgkmcnt(4)" : "+v"(vr[cur][0]), "+v"(vr[cur][1]), "+v"(vr[cur][2]), "+v"(vr[cur][3])); } \
        else asm volatile("s_waitcnt lgkmcnt(0)" : "+v"(vr[cur][0]), "+v"(vr[cur][1]), "+v"(vr[cur][2]), "+v"(vr[cur][3])); \
        u32x4 pw; pw.x = pk_bf16(s[2 * (cc)][0], s[2 * (cc)][1]); pw.y = pk_bf16(s[2 * (cc)][2], s[2 * (cc)][3]); \
        if constexpr ((cc) < 8) { pw.z = pk_bf16(s[2 * (cc) + 1][0], s[2 * (cc) + 1][1]); pw.w = pk_bf16(s[2 * (cc) + 1][2], s[2 * (cc) + 1][3]); } else { pw.z = 0u; pw.w = 0u; } \
        const bf16x8 pf = __builtin_bit_cast(bf16x8, pw); \
        osum = __builtin_amdgcn_mfma_f32_16x16x32_bf16(ones, pf, osum, 0, 0, 0); \
        oacc[0] = __builtin_amdgcn_mfma_f32_16x16x32_bf16(__builtin_shufflevector(vr[cur][0], vr[cur][1], 0, 1, 2, 3, 4, 5, 6, 7), pf, oacc[0], 0, 0, 0); \
        oacc[1] = __builtin_amdgcn_mfma_f32_16x16x32_bf16(__builtin_shufflevector(vr[cur][2], vr[cur][3], 0, 1, 2, 3, 4, 5, 6, 7), pf, oacc[1], 0, 0, 0); } while (0)
    V_READ(0, 0);
    V_STEP(0); V_STEP(1); V_STEP(2); V_STEP(3); V_STEP(4); V_STEP(5); V_STEP(6); V_STEP(7); V_STEP(8);
#undef V_READ
#undef V_STEP
    const float inv = 1.0f / osum[0];
#pragma unroll
    for (int dt = 0; dt < 2; ++dt) {
        u32x2 w; w.x = pk_bf16(oacc[dt][0] * inv, oacc[dt][1] * inv); w.y = pk_bf16(oacc[dt][2] * inv, oacc[dt][3] * inv);
        *(u32x2*)(o + (size_t)qrow * D_ + head * 32 + dt * 16 + 4 * fq) = w;
    }
    asm volatile("" :: "v"(ctv));
}

extern "C" void kernel_launch(void* const* d_in, const int* in_sizes, int n_in, void* d_out, int out_size, void* d_ws, size_t ws_size,
                              hipStream_t stream) {
    const bf16_t* x = (const bf16_t*)d_in[0];
    const bf16_t* wqkv = (const bf16_t*)d_in[1];
    const bf16_t* wo = (const bf16_t*)d_in[2];
    const bf16_t* w1 = (const bf16_t*)d_in[4];
    const bf16_t* w3 = (const bf16_t*)d_in[5];
    const bf16_t* w2 = (const bf16_t*)d_in[6];
    bf16_t* out = (bf16_t*)d_out;
    char* ws = (char*)d_ws;
    bf16_t* xn = (bf16_t*)(ws);
    bf16_t* qkv = (bf16_t*)(ws + (size_t)4 * 1024 * 1024);
    bf16_t* o = (bf16_t*)(ws + (size_t)20 * 1024 * 1024);
    bf16_t* h = (bf16_t*)(ws + (size_t)24 * 1024 * 1024);
    bf16_t* hn = (bf16_t*)(ws + (size_t)28 * 1024 * 1024);
    bf16_t* act = (bf16_t*)(ws + (size_t)32 * 1024 * 1024);

    {
        const int shm = 3 * (128 + 192) * 128;
        (void)hipFuncSetAttribute((const void*)k_qkv, hipFuncAttributeMaxDynamicSharedMemorySize, shm);
        k_qkv<<<256, 512, shm, stream>>>(QKV_LNF ? x : xn, wqkv, wo, qkv);
    }
    k_attn<<<32 * (S_ / AT_QB), AT_NW * 64, 0, stream>>>(qkv, o);
    {
        const int shm = 6 * (128 + 64) * 128;
        (void)hipFuncSetAttribute((const void*)k_wo, hipFuncAttributeMaxDynamicSharedMemorySize, shm);
        k_wo<<<256, 512, shm, stream>>>(o, wo, x, h);
    }
    k_ln<<<S_ / 4, 256, 0, stream>>>(h, hn, (const unsigned*)w2, 0u, (const unsigned*)w2, 0u);
    {
        const int shm = 8 * 16384;
        (void)hipFuncSetAttribute((const void*)k_gu, hipFuncAttributeMaxDynamicSharedMemorySize, shm);
        k_gu<<<256, 512, shm, stream>>>(hn, w1, w3, act, (const unsigned*)w2);
    }
    {
        const int shm = 6 * (128 + 64) * 128;
        (void)hipFuncSetAttribute((const void*)k_down, hipFuncAttributeMaxDynamicSharedMemorySize, shm);
        k_down<<<256, 512, shm, stream>>>(act, w2, h, out);
    }
}
```

```cpp
#include <hip/hip_runtime.h>
#include <hip/hip_bf16.h>
#include <stdint.h>

typedef unsigned short bf16_t;
typedef short bf16x8 __attribute__((ext_vector_type(8)));
typedef short s16x4 __attribute__((ext_vector_type(4)));
typedef float f32x4 __attribute__((ext_vector_type(4)));
typedef unsigned u32x4 __attribute__((ext_vector_type(4)));
typedef unsigned u32x2 __attribute__((ext_vector_type(2)));
#define LAS __attribute__((address_space(3)))
#define DI __device__ __forceinline__

constexpr int S_ = 2048, D_ = 1024, F_ = 3584, H_ = 32, DH_ = 32, WIN_ = 256;

DI unsigned pk_bf16(float lo, float hi) {
    typedef __bf16 bf2 __attribute__((ext_vector_type(2)));
    bf2 v; v.x = (__bf16)lo; v.y = (__bf16)hi;
    return __builtin_bit_cast(unsigned, v);
}
template <int OFF> DI s16x4 tr_read_a(unsigned addr) { s16x4 r; asm volatile("ds_read_b64_tr_b16 %0, %1 offset:%2" : "=v"(r) : "v"(addr), "i"(OFF)); return r; }
DI void glds16(const void* g, LAS unsigned char* l) {
    __builtin_amdgcn_global_load_lds((const __attribute__((address_space(1))) unsigned*)g, (LAS unsigned*)l, 16, 0, 0);
}
#define WAIT_V(n) asm volatile("s_waitcnt vmcnt(" #n ")" ::: "memory")
#define WAIT_L(n) asm volatile("s_waitcnt lgkmcnt(" #n ")" ::: "memory")
#define BAR __builtin_amdgcn_s_barrier()
#define SCHED __builtin_amdgcn_sched_barrier(0)
#define STAMP_DECL(kid)
#define STAMP_AT()
#define STAMP_RT()
#define STAMP_RT2()

#define CODE_TOUCH(SYM, NB, ACC) do { unsigned lo_, hi_; \
        asm volatile("s_getpc_b64 s[20:21]\n\ts_add_u32 s20, s20, " #SYM "@rel32@lo+4\n\ts_addc_u32 s21, s21, " #SYM "@rel32@hi+12\n\ts_mov_b32 %0, s20\n\ts_mov_b32 %1, s21" \
                     : "=s"(lo_), "=s"(hi_) :: "s20", "s21", "scc"); \
        const unsigned o_ = (threadIdx.x & 63) * 128u; \
        if (o_ < (NB)) ACC += *(const __attribute__((address_space(1))) unsigned*)((((unsigned long long)hi_ << 32) | lo_) + o_); } while (0)
extern "C" __global__ __launch_bounds__(256) void k_ln(const bf16_t* __restrict__ in, bf16_t* __restrict__ out, const unsigned* __restrict__ pf, unsigned pf_lines,
                                                 const unsigned* __restrict__ pfb, unsigned pfb_lines) {
    const int row = blockIdx.x * 4 + (threadIdx.x >> 6), lane = threadIdx.x & 63;
    const u32x4* p = (const u32x4*)(in + (size_t)row * D_);
    u32x4 a = p[lane], b = p[lane + 64];
    unsigned pfv = 0;
    if (blockIdx.x < 8 && threadIdx.x < 64) { CODE_TOUCH(k_gu, 6400, pfv); CODE_TOUCH(k_down, 3328, pfv); }
    { const unsigned li = blockIdx.x * 256u + threadIdx.x; if (li < pf_lines) pfv += pf[(size_t)li * 32];
      else if (li - pf_lines < pfb_lines) pfv += pfb[(size_t)(li - pf_lines) * 32]; }
    float v[16];
#pragma unroll
    for (int i = 0; i < 4; ++i) { v[2 * i] = __uint_as_float(a[i] << 16); v[2 * i + 1] = __uint_as_float(a[i] & 0xffff0000u);
                                  v[8 + 2 * i] = __uint_as_float(b[i] << 16); v[8 + 2 * i + 1] = __uint_as_float(b[i] & 0xffff0000u); }
    float s = 0.f;
#pragma unroll
    for (int i = 0; i < 16; ++i) s += v[i];
#pragma unroll
    for (int o = 32; o >= 1; o >>= 1) s += __shfl_xor(s, o);
    const float mean = s * (1.0f / D_);
    float q = 0.f;
#pragma unroll
    for (int i = 0; i < 16; ++i) { v[i] -= mean; q += v[i] * v[i]; }
#pragma unroll
    for (int o = 32; o >= 1; o >>= 1) q += __shfl_xor(q, o);
    const float rstd = 1.0f / sqrtf(q * (1.0f / D_) + 1e-5f);
    u32x4 oa, ob;
#pragma unroll
    for (int i = 0; i < 4; ++i) { oa[i] = pk_bf16(v[2 * i] * rstd, v[2 * i + 1] * rstd); ob[i] = pk_bf16(v[8 + 2 * i] * rstd, v[8 + 2 * i + 1] * rstd); }
    u32x4* po = (u32x4*)(out + (size_t)row * D_);
    po[lane] = oa; po[lane + 64] = ob;
    asm volatile("" :: "v"(pfv));
}

DI int bx_of_k(int k) { return ((k >> 1) & 1) | (((k >> 3) & 1) << 1); }

enum { EPI_QKV = 0, EPI_RES = 1 };
#define QKV_LNF 1
DI u32x4 pair_swap(u32x2 t0, u32x2 t1) {
    auto a = __builtin_amdgcn_permlane16_swap(t0.x, t1.x, false, false);
    auto b = __builtin_amdgcn_permlane16_swap(t0.y, t1.y, false, false);
    return (u32x4){a[0], b[0], a[1], b[1]};
}

template <int N> DI void wait_v() { asm volatile("s_waitcnt vmcnt(%0)" :: "i"(N) : "memory"); }
template <int OFF> DI bf16x8 ld128_a(unsigned addr) { bf16x8 r; asm volatile("ds_read_b128 %0, %1 offset:%2" : "=v"(r) : "v"(addr), "i"(OFF)); return r; }

template <int BM, int BN, int WM, int WN, int NBUF, int EPI, int RM, int RN, int K>
DI void gemm_pp(const bf16_t* __restrict__ A, int lda, const bf16_t* __restrict__ W, int ldb,
                const bf16_t* __restrict__ res, bf16_t* __restrict__ out, int tiles_m) {
    extern __shared__ __attribute__((aligned(16))) unsigned char smem_raw[];
    LAS unsigned char* lds = (LAS unsigned char*)smem_raw;
    constexpr int MT = BM / WM / 16, NT = BN / WN / 16;
    constexpr int NPA = BM / 64, NPB = BN / 64, NP = NPA + NPB;
    constexpr int ABYTES = BM * 128, TILE_BYTES = (BM + BN) * 128;
    constexpr int nt = K / 64;
    constexpr bool LNF = (EPI == EPI_QKV) && QKV_LNF;
    static_assert(WM * WN == 8, "8 waves");
    static_assert(MT == 2 || MT == 4, "MT");
    static_assert(!LNF || (MT == 4 && WN == 4 && WM == 2), "LN fold geometry");
    STAMP_DECL(EPI == EPI_QKV ? 0 : (K == 1024 ? 1 : 2)); STAMP_RT(); STAMP_AT();
    const int tid = threadIdx.x, wid = __builtin_amdgcn_readfirstlane(tid >> 6), lane = tid & 63;
    const int wm = wid / WN, wn = wid % WN, fr = lane & 15, fq = lane >> 4;
    int tm, tn;
    { const int bid = blockIdx.x, xcd = bid & 7, idx = bid >> 3; const int regm = tiles_m / RM;
      const int rr = xcd % regm, rc = xcd / regm; tm = rr * RM + (idx % RM); tn = rc * RN + (idx / RM); }
    const int m0 = tm * BM, n0 = tn * BN;
    const bf16_t* asrc[NPA]; const bf16_t* bsrc[NPB];
#pragma unroll
    for (int i = 0; i < NPA; ++i) { const int row = (wid + 8 * i) * 8 + (lane >> 3), ch = (lane & 7) ^ ((row >> 1) & 7);
        asrc[i] = A + (size_t)(m0 + row) * lda + ch * 8; }
#pragma unroll
    for (int j = 0; j < NPB; ++j) { const int k = wid * 8 + (lane >> 3), s16 = lane & 7, n16 = (s16 >> 1) ^ bx_of_k(k);
        bsrc[j] = W + (size_t)k * ldb + n0 + j * 64 + n16 * 16 + (s16 & 1) * 8; }
    const size_t bstep = (size_t)64 * ldb;
    auto stage = [&](int t, int buf) {
        LAS unsigned char* base = lds + buf * TILE_BYTES + wid * 1024;
#pragma unroll
        for (int i = 0; i < NPA; ++i) glds16(asrc[i] + (size_t)t * 64, base + i * 8192);
#pragma unroll
        for (int j = 0; j < NPB; ++j) glds16(bsrc[j] + (size_t)t * bstep, base + ABYTES + j * 8192);
    };
    const int rotm = LNF ? wn : 0, krot = LNF ? wm : 0;
    const int sw = (fr >> 1) & 7;
    unsigned aadr[MT][2], badr[NT][2];
#pragma unroll
    for (int j = 0; j < MT; ++j)
#pragma unroll
        for (int sl = 0; sl < 2; ++sl) aadr[j][sl] = (unsigned)(size_t)lds + (wm * MT * 16 + ((j + rotm) & (MT - 1)) * 16 + fr) * 128 + (((((sl ^ krot) * 4) + fq) ^ sw) * 16);
    { const int q = (lane & 15) >> 2, p = lane & 3, xl = ((q >> 1) & 1) | ((fq & 1) << 1);
#pragma unroll
      for (int n = 0; n < NT; ++n) { const int ntg = wn * NT + n;
          const unsigned b0 = (unsigned)(size_t)lds + ABYTES + (ntg >> 2) * 8192 + (8 * fq + q) * 128 + (((ntg & 3) ^ xl) * 32) + p * 8;
          badr[n][0] = b0 + krot * 4096; badr[n][1] = b0 + (1 - krot) * 4096; } }
    f32x4 acc[MT][NT];
#pragma unroll
    for (int m = 0; m < MT; ++m)
#pragma unroll
        for (int n = 0; n < NT; ++n) acc[m][n] = (f32x4){0.f, 0.f, 0.f, 0.f};
    f32x4 rsacc = (f32x4){0.f, 0.f, 0.f, 0.f}, rqacc = rsacc, csacc[NT];
#pragma unroll
    for (int n = 0; n < NT; ++n) csacc[n] = rsacc;
    const bf16x8 ones = (bf16x8){0x3F80, 0x3F80, 0x3F80, 0x3F80, 0x3F80, 0x3F80, 0x3F80, 0x3F80};
    bf16x8 afA[MT][2], afB[MT][2]; s16x4 bA[NT][4], bB[NT][4];
#define P_READ(AF, BR, buf) do { const unsigned bo_ = (unsigned)((buf) * TILE_BYTES); \
        if constexpr (LNF) { \
            _Pragma("unroll") for (int n = 0; n < NT; ++n) { const unsigned b0_ = badr[n][0] + bo_, b1_ = badr[n][1] + bo_; \
                BR[n][0] = tr_read_a<0>(b0_); BR[n][1] = tr_read_a<512>(b0_); BR[n][2] = tr_read_a<0>(b1_); BR[n][3] = tr_read_a<512>(b1_); } \
            _Pragma("unroll") for (int j = 0; j < MT; ++j) { AF[j][0] = ld128_a<0>(aadr[j][0] + bo_); AF[j][1] = ld128_a<0>(aadr[j][1] + bo_); } \
        } else { \
            _Pragma("unroll") for (int n = 0; n < NT; ++n) { const unsigned ba = badr[n][0] + bo_; \
                BR[n][0] = tr_read_a<0>(ba); BR[n][1] = tr_read_a<512>(ba); BR[n][2] = tr_read_a<4096>(ba); BR[n][3] = tr_read_a<4608>(ba); } \
            const unsigned a0_ = aadr[0][0] + bo_, a1_ = aadr[0][1] + bo_; \
            AF[0][0] = ld128_a<0>(a0_); AF[0][1] = ld128_a<0>(a1_); AF[1][0] = ld128_a<2048>(a0_); AF[1][1] = ld128_a<2048>(a1_); \
            if constexpr (MT == 4) { AF[MT - 2][0] = ld128_a<4096>(a0_); AF[MT - 2][1] = ld128_a<4096>(a1_); AF[MT - 1][0] = ld128_a<6144>(a0_); AF[MT - 1][1] = ld128_a<6144>(a1_); } } } while (0)
#define P_WAITL(AF, BR) do { _Pragma("unroll") for (int n = 0; n < NT; ++n) \
        asm volatile("s_waitcnt lgkmcnt(0)" : "+v"(BR[n][0]), "+v"(BR[n][1]), "+v"(BR[n][2]), "+v"(BR[n][3]) :: "memory"); \
        _Pragma("unroll") for (int m = 0; m < MT; ++m) asm volatile("" : "+v"(AF[m][0]), "+v"(AF[m][1])); } while (0)
#define P_MMA(AF, BR) do { _Pragma("unroll") for (int kk = 0; kk < 2; ++kk) _Pragma("unroll") for (int m = 0; m < MT; ++m) _Pragma("unroll") for (int n = 0; n < NT; ++n) \
        acc[m][n] = __builtin_amdgcn_mfma_f32_16x16x32_bf16(__builtin_shufflevector(BR[n][2 * kk], BR[n][2 * kk + 1], 0, 1, 2, 3, 4, 5, 6, 7), AF[m][kk], acc[m][n], 0, 0, 0); \
        if constexpr (LNF) { _Pragma("unroll") for (int kk = 0; kk < 2; ++kk) { rsacc = __builtin_amdgcn_mfma_f32_16x16x32_bf16(ones, AF[0][kk], rsacc, 0, 0, 0); \
                rqacc = __builtin_amdgcn_mfma_f32_16x16x32_bf16(AF[0][kk], AF[0][kk], rqacc, 0, 0, 0); } \
            _Pragma("unroll") for (int n = 0; n < NT; ++n) csacc[n] = __builtin_amdgcn_mfma_f32_16x16x32_bf16(__builtin_shufflevector(BR[n][0], BR[n][1], 0, 1, 2, 3, 4, 5, 6, 7), ones, csacc[n], 0, 0, 0); } } while (0)
#define P_STEP(AFC, BRC, AFN, BRN, FULL, WN_, RESLD) do { \
        P_WAITL(AFC, BRC); \
        wait_v<WN_>(); \
        BAR; \
        const int nb_ = (buf + 1 == NBUF) ? 0 : buf + 1; \
        if (FULL || t + 1 < nt) P_READ(AFN, BRN, nb_); \
        if (FULL || t + NBUF < nt) stage(t + NBUF, buf); \
        if (RESLD) { _Pragma("unroll") for (int m = 0; m < MT; ++m) _Pragma("unroll") for (int n = 0; n < NT; ++n) \
            rres[m][n] = *(const u32x2*)(res + (size_t)(m0 + (wm * MT + m) * 16 + fr) * D_ + n0 + (wn * NT + n) * 16 + 4 * fq); } \
        P_MMA(AFC, BRC); \
        buf = nb_; ++t; } while (0)
    u32x2 rres[MT][NT];
    constexpr int NR = (EPI == EPI_RES) ? MT * NT : 0;
#pragma unroll
    for (int t = 0; t < NBUF; ++t) stage(t, t);
    wait_v<(NBUF - 1) * NP>();
    BAR;
    STAMP_AT();
    P_READ(afA, bA, 0);
    int buf = 0, t = 0;
    constexpr int nmain = ((nt - NBUF) / 2) * 2;
    for (; t < nmain;) {
        P_STEP(afA, bA, afB, bB, true, (NBUF - 2) * NP, false);
        P_STEP(afB, bB, afA, bA, true, (NBUF - 2) * NP, false);
    }
#define P_TAILW(tt) ((((tt) + NBUF - 1 < nt - 1 ? (tt) + NBUF - 1 : nt - 1) - ((tt) + 1) > 0 ? (((tt) + NBUF - 1 < nt - 1 ? (tt) + NBUF - 1 : nt - 1) - ((tt) + 1)) : 0) * NP + ((tt) > nt - NBUF ? NR : 0))
#define P_TAIL2(tt) do { t = (tt); P_STEP(afA, bA, afB, bB, false, P_TAILW(tt), (NR > 0 && (tt) == nt - NBUF)); \
        P_STEP(afB, bB, afA, bA, false, P_TAILW((tt) + 1), (NR > 0 && (tt) + 1 == nt - NBUF)); } while (0)
    if constexpr (nmain + 0 < nt) P_TAIL2(nmain + 0);
    if constexpr (nmain + 2 < nt) P_TAIL2(nmain + 2);
    if constexpr (nmain + 4 < nt) P_TAIL2(nmain + 4);
    if constexpr (nmain + 6 < nt) P_TAIL2(nmain + 6);
    static_assert(nmain + 8 >= nt, "tail");
#undef P_TAILW
#undef P_TAIL2
    STAMP_AT();
#undef P_READ
#undef P_WAITL
#undef P_MMA
#undef P_STEP
    if constexpr (EPI == EPI_RES) {
#pragma unroll
        for (int m = 0; m < MT; ++m)
#pragma unroll
            for (int n = 0; n < NT; ++n) asm volatile("" : "+v"(rres[m][n]));
    }
    typedef float f32x2 __attribute__((ext_vector_type(2)));
    LAS f32x2* stat = (LAS f32x2*)lds;
    LAS float* cpart = (LAS float*)(lds + BM * 8);
    if constexpr (LNF) {
        const float rq = (fr & 2) ? ((fr & 1) ? rqacc[3] : rqacc[2]) : ((fr & 1) ? rqacc[1] : rqacc[0]);
        const float mean = rsacc[0] * (1.0f / K), var = fmaxf(rq * (1.0f / K) - mean * mean, 0.f);
        if (fq == (fr >> 2)) stat[wm * 64 + wn * 16 + fr] = (f32x2){mean, 1.0f / sqrtf(var + 1e-5f)};
#pragma unroll
        for (int n = 0; n < NT; ++n) if (fr == 0) *(LAS f32x4*)(cpart + wm * BN + (wn * NT + n) * 16 + 4 * fq) = csacc[n];
        __syncthreads();
    }
    unsigned pfv = 0;
    if constexpr (EPI == EPI_QKV) { const unsigned li = blockIdx.x * 512u + threadIdx.x; if (li < (unsigned)((size_t)D_ * D_ * 2 / 128)) pfv = ((const unsigned*)res)[(size_t)li * 32];
        if (blockIdx.x >= 248 && wid == 0) { CODE_TOUCH(k_attn, 3456, pfv); CODE_TOUCH(k_wo, 4992, pfv); } }
    const int pcol = (fq & 1) ? 16 + 4 * (fq - 1) : 4 * fq;
    f32x4 csum[NT];
    if constexpr (LNF) {
#pragma unroll
        for (int n = 0; n < NT; ++n) csum[n] = *(const LAS f32x4*)(cpart + (wn * NT + n) * 16 + 4 * fq) + *(const LAS f32x4*)(cpart + BN + (wn * NT + n) * 16 + 4 * fq);
    }
#pragma unroll
    for (int m = 0; m < MT; ++m) {
        const int lrow = (wm * MT + ((m + rotm) & (MT - 1))) * 16 + fr, row = m0 + lrow;
        f32x2 st; if constexpr (LNF) st = stat[lrow];
        u32x2 w[NT];
#pragma unroll
        for (int n = 0; n < NT; ++n) {
            f32x4 v = acc[m][n];
            if constexpr (LNF) v = (v - csum[n] * st.x) * st.y;
            if constexpr (EPI == EPI_RES) { const u32x2 r = rres[m][n];
                v[0] += __uint_as_float(r.x << 16); v[1] += __uint_as_float(r.x & 0xffff0000u);
                v[2] += __uint_as_float(r.y << 16); v[3] += __uint_as_float(r.y & 0xffff0000u); }
            w[n].x = pk_bf16(v[0], v[1]); w[n].y = pk_bf16(v[2], v[3]);
        }
#pragma unroll
        for (int n = 0; n + 1 < NT; n += 2) {
            const u32x4 o = pair_swap(w[n], w[n + 1]);
            const int col = n0 + (wn * NT + n) * 16 + pcol;
            if constexpr (EPI == EPI_QKV) *(u32x4*)(out + ((size_t)(col >> 5) * S_ + row) * 32 + (col & 31)) = o;
            else *(u32x4*)(out + (size_t)row * D_ + col) = o;
        }
        if constexpr (NT & 1) {
            const int col = n0 + (wn * NT + NT - 1) * 16 + 4 * fq;
            if constexpr (EPI == EPI_QKV) *(u32x2*)(out + ((size_t)(col >> 5) * S_ + row) * 32 + (col & 31)) = w[NT - 1];
            else *(u32x2*)(out + (size_t)row * D_ + col) = w[NT - 1];
        }
    }
    asm volatile("" :: "v"(pfv));
    WAIT_V(0); STAMP_AT(); STAMP_RT2();
}

extern "C" __global__ __launch_bounds__(512, 2) void k_qkv(const bf16_t* __restrict__ A, const bf16_t* __restrict__ W, const bf16_t* __restrict__ wo, bf16_t* __restrict__ out) {
    gemm_pp<128, 192, 2, 4, 3, EPI_QKV, 8, 4, 1024>(A, D_, W, 3 * D_, wo, out, 16);
}
extern "C" __global__ __launch_bounds__(512, 2) void k_wo(const bf16_t* __restrict__ A, const bf16_t* __restrict__ W, const bf16_t* __restrict__ res, bf16_t* __restrict__ out) {
    gemm_pp<128, 64, 4, 2, 6, EPI_RES, 4, 8, 1024>(A, D_, W, D_, res, out, 16);
}
extern "C" __global__ __launch_bounds__(512, 2) void k_down(const bf16_t* __restrict__ A, const bf16_t* __restrict__ W, const bf16_t* __restrict__ res, bf16_t* __restrict__ out) {
    gemm_pp<128, 64, 4, 2, 6, EPI_RES, 4, 8, 3584>(A, F_, W, D_, res, out, 16);
}

extern "C" __global__ __launch_bounds__(512, 2) void k_gu(const bf16_t* __restrict__ A, const bf16_t* __restrict__ W1, const bf16_t* __restrict__ W3,
                                                       bf16_t* __restrict__ act, const unsigned* __restrict__ pf) {
    extern __shared__ __attribute__((aligned(16))) unsigned char smem_raw[];
    LAS unsigned char* lds = (LAS unsigned char*)smem_raw;
    constexpr int K = D_, HTB = 16384;
    STAMP_DECL(3); STAMP_RT(); STAMP_AT();
    const int tid = threadIdx.x, wid = __builtin_amdgcn_readfirstlane(tid >> 6), lane = tid & 63;
    const int wr = wid >> 2, wc = wid & 3, fr = lane & 15, fq = lane >> 4;
    if (blockIdx.x >= 224) {
        constexpr unsigned NL = (unsigned)((size_t)F_ * D_ * 2 / 128);
        const unsigned l0 = (blockIdx.x - 224) * 512u + threadIdx.x;
        unsigned a = 0;
#pragma unroll
        for (int i = 0; i < 4; ++i) { const unsigned li = l0 + i * 16384u; if (li < NL) a += ((const unsigned*)W1)[(size_t)li * 32] + ((const unsigned*)W3)[(size_t)li * 32]; }
#pragma unroll
        for (int i = 0; i < 4; ++i) { const unsigned li = l0 + i * 16384u; if (li < NL) a += pf[(size_t)li * 32]; }
        if (threadIdx.x < 64) CODE_TOUCH(k_down, 3328, a);
        asm volatile("" :: "v"(a));
        return;
    }
    int tm, tn;
    { const int bid = blockIdx.x, xcd = bid & 7, idx = bid >> 3; tm = (xcd & 1) * 4 + (idx & 3); tn = (xcd >> 1) * 7 + (idx >> 2); }
    const int m0 = tm * 256, f0 = tn * 128;
    constexpr int nt = K / 64;
    unsigned aoffs[2], boffs[2];
#pragma unroll
    for (int i = 0; i < 2; ++i) { const int row = (wid + 8 * i) * 8 + (lane >> 3), ch = (lane & 7) ^ ((row >> 1) & 7);
        aoffs[i] = (unsigned)(row * K + ch * 8) * 2u;
        const int k = wid * 8 + (lane >> 3), s16 = lane & 7, n16 = (s16 >> 1) ^ bx_of_k(k);
        boffs[i] = (unsigned)(k * F_ + i * 64 + n16 * 16 + (s16 & 1) * 8) * 2u; }
    const char* gA0 = (const char*)(A + (size_t)m0 * K); const char* gA1 = gA0 + (size_t)128 * K * 2;
    const char* gB0 = (const char*)(W1 + f0); const char* gB1 = (const char*)(W3 + f0);
    constexpr size_t akstep = 128, bkstep = (size_t)64 * F_ * 2;
    const unsigned ldsw = (unsigned)wid * 1024u;
#define G_SA(b, h) (((b) * 2 + (h)) * HTB)
#define G_SB(b, h) ((4 + (b) * 2 + (h)) * HTB)
#define G_STAGE(bufoff, gbase, voff) do { _Pragma("unroll") for (int _i = 0; _i < 2; ++_i) glds16((gbase) + (voff)[_i], lds + (bufoff) + ldsw + _i * 8192); } while (0)
    const int sw = (fr >> 1) & 7;
    const int aoff0 = (wr * 64 + fr) * 128 + (((wr * 4 + fq) ^ sw) * 16), aoff1 = aoff0 ^ 64;
    unsigned badr[2][2];
    { const int q = (lane & 15) >> 2, p = lane & 3, xl = ((q >> 1) & 1) | ((fq & 1) << 1);
#pragma unroll
      for (int n = 0; n < 2; ++n) { const int ntg = wc * 2 + n;
          const unsigned b0 = (unsigned)(size_t)lds + 4 * HTB + (ntg >> 2) * 8192 + (8 * fq + q) * 128 + (((ntg & 3) ^ xl) * 32) + p * 8;
          badr[n][0] = b0 + wr * 4096; badr[n][1] = b0 + (1 - wr) * 4096; } }
#define G_LDA(dst, b, h) do { _Pragma("unroll") for (int m = 0; m < 4; ++m) { dst[m][0] = *(const LAS bf16x8*)(lds + G_SA(b, h) + aoff0 + m * 2048); \
        dst[m][1] = *(const LAS bf16x8*)(lds + G_SA(b, h) + aoff1 + m * 2048); } } while (0)
#define G_LDB(dst, bo, h) do { _Pragma("unroll") for (int n = 0; n < 2; ++n) { const unsigned b0_ = badr[n][0] + (bo), b1_ = badr[n][1] + (bo); \
        dst[n][0] = tr_read_a<(h) * HTB>(b0_); dst[n][1] = tr_read_a<(h) * HTB + 512>(b0_); \
        dst[n][2] = tr_read_a<(h) * HTB>(b1_); dst[n][3] = tr_read_a<(h) * HTB + 512>(b1_); } } while (0)
#define G_WAITB(R) do { asm volatile("s_waitcnt lgkmcnt(0)" : "+v"(R[0][0]), "+v"(R[0][1]), "+v"(R[0][2]), "+v"(R[0][3]), "+v"(R[1][0]), "+v"(R[1][1]), "+v"(R[1][2]), "+v"(R[1][3]) :: "memory"); } while (0)
#define G_FR(R, n, k) __builtin_shufflevector(R[n][2 * (k)], R[n][2 * (k) + 1], 0, 1, 2, 3, 4, 5, 6, 7)
#define G_MMA(ai, bj, At, Bt) do { __builtin_amdgcn_s_setprio(1); _Pragma("unroll") for (int m = 0; m < 4; ++m) _Pragma("unroll") for (int n = 0; n < 2; ++n) \
        _Pragma("unroll") for (int k = 0; k < 2; ++k) acc[ai][bj][m][n] = __builtin_amdgcn_mfma_f32_16x16x32_bf16(G_FR(Bt, n, k), At[m][k], acc[ai][bj][m][n], 0, 0, 0); \
        __builtin_amdgcn_s_setprio(0); } while (0)
    f32x4 acc[2][2][4][2];
#pragma unroll
    for (int a = 0; a < 2; ++a)
#pragma unroll
        for (int b = 0; b < 2; ++b)
#pragma unroll
            for (int m = 0; m < 4; ++m)
#pragma unroll
                for (int n = 0; n < 2; ++n) acc[a][b][m][n] = (f32x4){0.f, 0.f, 0.f, 0.f};
    bf16x8 At[4][2]; s16x4 B0[2][4], B1[2][4];
    G_STAGE(G_SB(0, 0), gB0, boffs); G_STAGE(G_SA(0, 0), gA0, aoffs); G_STAGE(G_SB(0, 1), gB1, boffs); G_STAGE(G_SA(0, 1), gA1, aoffs);
    if (wr == 1) BAR;
    WAIT_V(4); BAR;
    G_STAGE(G_SB(1, 0), gB0 + bkstep, boffs); G_STAGE(G_SA(1, 0), gA0 + akstep, aoffs); G_STAGE(G_SB(1, 1), gB1 + bkstep, boffs);
    WAIT_V(6); BAR;
    STAMP_AT();
    for (int t = 0; t < nt; t += 2) {
        const int t1 = t + 1, t2 = (t + 2 < nt) ? t + 2 : nt - 1, t3 = (t + 3 < nt) ? t + 3 : nt - 1;
        G_LDB(B0, 0, 0); SCHED; G_LDA(At, 0, 0); G_STAGE(G_SA(1, 1), gA1 + t1 * akstep, aoffs);
        WAIT_L(8); BAR; G_WAITB(B0); G_MMA(0, 0, At, B0); BAR; SCHED;
        G_LDB(B1, 0, 1); G_STAGE(G_SB(0, 0), gB0 + t2 * bkstep, boffs);
        BAR; G_WAITB(B1); G_MMA(0, 1, At, B1); BAR;
        G_LDA(At, 0, 1); G_STAGE(G_SA(0, 0), gA0 + t2 * akstep, aoffs);
        BAR; WAIT_L(0); G_MMA(1, 0, At, B0); BAR; SCHED;
        G_STAGE(G_SB(0, 1), gB1 + t2 * bkstep, boffs);
        WAIT_V(6); BAR; G_MMA(1, 1, At, B1); BAR;
        G_LDB(B0, 2 * HTB, 0); SCHED; G_LDA(At, 1, 0); G_STAGE(G_SA(0, 1), gA1 + t2 * akstep, aoffs);
        WAIT_L(8); BAR; G_WAITB(B0); G_MMA(0, 0, At, B0); BAR; SCHED;
        G_LDB(B1, 2 * HTB, 1); G_STAGE(G_SB(1, 0), gB0 + t3 * bkstep, boffs);
        BAR; G_WAITB(B1); G_MMA(0, 1, At, B1); BAR;
        G_LDA(At, 1, 1); G_STAGE(G_SA(1, 0), gA0 + t3 * akstep, aoffs);
        BAR; WAIT_L(0); G_MMA(1, 0, At, B0); BAR; SCHED;
        G_STAGE(G_SB(1, 1), gB1 + t3 * bkstep, boffs);
        WAIT_V(6); BAR; G_MMA(1, 1, At, B1); BAR;
    }
    STAMP_AT();
    WAIT_V(0);
    if (wr == 0) BAR;
    const int pcol = (fq & 1) ? 16 + 4 * (fq - 1) : 4 * fq;
#pragma unroll
    for (int ai = 0; ai < 2; ++ai)
#pragma unroll
        for (int m = 0; m < 4; ++m) {
            const int lrow = ai * 128 + wr * 64 + m * 16 + fr, row = m0 + lrow;
            u32x2 w[2];
#pragma unroll
            for (int n = 0; n < 2; ++n) {
                const f32x4 g = acc[ai][0][m][n], u = acc[ai][1][m][n];
                float o[4];
#pragma unroll
                for (int j = 0; j < 4; ++j) o[j] = g[j] * __builtin_amdgcn_rcpf(1.0f + __builtin_amdgcn_exp2f(g[j] * -1.4426950408889634f)) * u[j];
                w[n].x = pk_bf16(o[0], o[1]); w[n].y = pk_bf16(o[2], o[3]);
            }
            *(u32x4*)(act + (size_t)row * F_ + f0 + wc * 32 + pcol) = pair_swap(w[0], w[1]);
        }
    WAIT_V(0); STAMP_AT(); STAMP_RT2();
#undef G_SA
#undef G_SB
#undef G_STAGE
#undef G_LDA
#undef G_LDB
#undef G_MMA
#undef G_WAITB
#undef G_FR
}

constexpr int AT_QB = 256, AT_ROWS = AT_QB + 256, AT_NW = AT_QB / 16;
extern "C" __global__ __launch_bounds__(AT_NW * 64, 4) void k_attn(const bf16_t* __restrict__ qkv, bf16_t* __restrict__ o) {
    __shared__ __attribute__((aligned(16))) unsigned char sm[2 * AT_ROWS * 64];
    LAS unsigned char* lk = (LAS unsigned char*)sm; LAS unsigned char* lv = lk + AT_ROWS * 64;
    const int tid = threadIdx.x, wid = __builtin_amdgcn_readfirstlane(tid >> 6), lane = tid & 63, fr = lane & 15, fq = lane >> 4;
    const int head = blockIdx.x & 31, qb = blockIdx.x >> 5, q0 = qb * AT_QB;
    const bf16_t* Q = qkv + (size_t)head * S_ * 32;
    const bf16_t* Kg = qkv + (size_t)(32 + head) * S_ * 32;
    const bf16_t* Vg = qkv + (size_t)(64 + head) * S_ * 32;
    static_assert(AT_ROWS / 16 == 2 * AT_NW, "two pieces per wave");
#pragma unroll
    for (int i = 0; i < 2; ++i) { const int pc = wid + i * AT_NW, row = pc * 16 + (lane >> 2), slot = lane & 3;
        int key = q0 - 256 + row; key = key < 0 ? 0 : (key > S_ - 1 ? S_ - 1 : key);
        glds16(Kg + (size_t)key * 32 + (slot ^ ((-(row >> 2)) & 3)) * 8, lk + pc * 1024); }
    const int qrow = q0 + wid * 16 + fr;
    bf16x8 qf;
    asm volatile("global_load_dwordx4 %0, %1, off" : "=v"(qf) : "v"(Q + (size_t)qrow * 32 + fq * 8) : "memory");
#pragma unroll
    for (int i = 0; i < 2; ++i) { const int pc = wid + i * AT_NW, row = pc * 16 + (lane >> 2), slot = lane & 3;
        int key = q0 - 256 + row; key = key < 0 ? 0 : (key > S_ - 1 ? S_ - 1 : key);
        glds16(Vg + (size_t)key * 32 + (slot ^ (((row >> 2) & 1) << 1)) * 8, lv + pc * 1024); }
    asm volatile("s_waitcnt vmcnt(2)" : "+v"(qf) :: "memory");
    __builtin_amdgcn_s_barrier();
    unsigned ctv = 0;
    if (blockIdx.x < 8 && wid == 0) { CODE_TOUCH(k_wo, 4992, ctv); CODE_TOUCH(k_ln, 1152, ctv); CODE_TOUCH(k_gu, 6400, ctv); }
    const int kbase = q0 + wid * 16 - 256;
    const int istart = kbase < 0 ? (-kbase) >> 4 : 0;
    f32x4 s[17];
    const int koff = (wid * 16 + fr) * 64 + ((fq ^ ((-(fr >> 2)) & 3)) * 16);
#pragma unroll
    for (int i = 0; i < 17; ++i) {
        const bf16x8 kf = *(const LAS bf16x8*)(lk + koff + i * 1024);
        s[i] = __builtin_amdgcn_mfma_f32_16x16x32_bf16(kf, qf, (f32x4){0.f, 0.f, 0.f, 0.f}, 0, 0, 0);
    }
    asm volatile("s_nop 7\n\ts_nop 7\n\ts_nop 3" : "+v"(s[0]), "+v"(s[1]), "+v"(s[2]), "+v"(s[3]), "+v"(s[4]), "+v"(s[5]), "+v"(s[6]), "+v"(s[7]), "+v"(s[8]),
                 "+v"(s[9]), "+v"(s[10]), "+v"(s[11]), "+v"(s[12]), "+v"(s[13]), "+v"(s[14]), "+v"(s[15]), "+v"(s[16]));
    const float NEG = -3.0e38f;
#pragma unroll
    for (int r = 0; r < 4; ++r) { if (!(4 * fq + r > fr)) s[0][r] = NEG; if (!(4 * fq + r <= fr)) s[16][r] = NEG; }
    if (istart > 0) {
#pragma unroll
        for (int i = 0; i < 16; ++i) if (i < istart) s[i] = (f32x4){NEG, NEG, NEG, NEG};
    }
    float mx = NEG;
#pragma unroll
    for (int i = 0; i < 17; ++i) { asm("v_max3_f32 %0, %1, %2, %3" : "=v"(mx) : "v"(mx), "v"(s[i][0]), "v"(s[i][1]));
                                   asm("v_max3_f32 %0, %1, %2, %3" : "=v"(mx) : "v"(mx), "v"(s[i][2]), "v"(s[i][3])); }
    mx = fmaxf(mx, __shfl_xor(mx, 16)); mx = fmaxf(mx, __shfl_xor(mx, 32));
    const float c = 0.17677669529663687f * 1.4426950408889634f;
    const float mc = -mx * c;
    typedef float f32x2 __attribute__((ext_vector_type(2)));
    const f32x2 c2 = {c, c}, mc2 = {mc, mc};
#pragma unroll
    for (int i = 0; i < 17; ++i) {
        const f32x2 lo = __builtin_elementwise_fma((f32x2){s[i][0], s[i][1]}, c2, mc2), hi = __builtin_elementwise_fma((f32x2){s[i][2], s[i][3]}, c2, mc2);
        s[i][0] = __builtin_amdgcn_exp2f(lo.x); s[i][1] = __builtin_amdgcn_exp2f(lo.y); s[i][2] = __builtin_amdgcn_exp2f(hi.x); s[i][3] = __builtin_amdgcn_exp2f(hi.y);
    }
    f32x4 oacc[2] = {(f32x4){0.f, 0.f, 0.f, 0.f}, (f32x4){0.f, 0.f, 0.f, 0.f}};
    f32x4 osum = (f32x4){0.f, 0.f, 0.f, 0.f};
    const bf16x8 ones = (bf16x8){0x3F80, 0x3F80, 0x3F80, 0x3F80, 0x3F80, 0x3F80, 0x3F80, 0x3F80};
    const int q4 = (lane & 15) >> 2, p4 = lane & 3;
    const unsigned va0 = (unsigned)(size_t)lv + (wid * 16 + 4 * fq + q4) * 64 + p4 * 8 + ((fq & 1) << 5), va1 = va0 ^ 32;
    s16x4 vr[2][4];
#define V_READ(SET, cc) do { vr[SET][0] = tr_read_a<(cc) * 2048>(va0); vr[SET][1] = tr_read_a<(cc) * 2048 + ((cc) < 8 ? 1024 : 0)>(va0); \
        vr[SET][2] = tr_read_a<(cc) * 2048>(va1); vr[SET][3] = tr_read_a<(cc) * 2048 + ((cc) < 8 ? 1024 : 0)>(va1); } while (0)
#define V_STEP(cc) do { constexpr int cur = (cc) & 1; \
        if constexpr ((cc) < 8) { V_READ(cur ^ 1, (cc) + 1); asm volatile("s_waitcnt lgkmcnt(4)" : "+v"(vr[cur][0]), "+v"(vr[cur][1]), "+v"(vr[cur][2]), "+v"(vr[cur][3])); } \
        else asm volatile("s_waitcnt lgkmcnt(0)" : "+v"(vr[cur][0]), "+v"(vr[cur][1]), "+v"(vr[cur][2]), "+v"(vr[cur][3])); \
        u32x4 pw; pw.x = pk_bf16(s[2 * (cc)][0], s[2 * (cc)][1]); pw.y = pk_bf16(s[2 * (cc)][2], s[2 * (cc)][3]); \
        if constexpr ((cc) < 8) { pw.z = pk_bf16(s[2 * (cc) + 1][0], s[2 * (cc) + 1][1]); pw.w = pk_bf16(s[2 * (cc) + 1][2], s[2 * (cc) + 1][3]); } else { pw.z = 0u; pw.w = 0u; } \
        const bf16x8 pf = __builtin_bit_cast(bf16x8, pw); \
        osum = __builtin_amdgcn_mfma_f32_16x16x32_bf16(ones, pf, osum, 0, 0, 0); \
        oacc[0] = __builtin_amdgcn_mfma_f32_16x16x32_bf16(__builtin_shufflevector(vr[cur][0], vr[cur][1], 0, 1, 2, 3, 4, 5, 6, 7), pf, oacc[0], 0, 0, 0); \
        oacc[1] = __builtin_amdgcn_mfma_f32_16x16x32_bf16(__builtin_shufflevector(vr[cur][2], vr[cur][3], 0, 1, 2, 3, 4, 5, 6, 7), pf, oacc[1], 0, 0, 0); } while (0)
    asm volatile("s_waitcnt vmcnt(0)" ::: "memory");
    __builtin_amdgcn_s_barrier();
    V_READ(0, 0);
    V_STEP(0); V_STEP(1); V_STEP(2); V_STEP(3); V_STEP(4); V_STEP(5); V_STEP(6); V_STEP(7); V_STEP(8);
#undef V_READ
#undef V_STEP
    const float inv = 1.0f / osum[0];
#pragma unroll
    for (int dt = 0; dt < 2; ++dt) {
        u32x2 w; w.x = pk_bf16(oacc[dt][0] * inv, oacc[dt][1] * inv); w.y = pk_bf16(oacc[dt][2] * inv, oacc[dt][3] * inv);
        *(u32x2*)(o + (size_t)qrow * D_ + head * 32 + dt * 16 + 4 * fq) = w;
    }
    asm volatile("" :: "v"(ctv));
}

extern "C" void kernel_launch(void* const* d_in, const int* in_sizes, int n_in, void* d_out, int out_size, void* d_ws, size_t ws_size,
                              hipStream_t stream) {
    const bf16_t* x = (const bf16_t*)d_in[0];
    const bf16_t* wqkv = (const bf16_t*)d_in[1];
    const bf16_t* wo = (const bf16_t*)d_in[2];
    const bf16_t* w1 = (const bf16_t*)d_in[4];
    const bf16_t* w3 = (const bf16_t*)d_in[5];
    const bf16_t* w2 = (const bf16_t*)d_in[6];
    bf16_t* out = (bf16_t*)d_out;
    char* ws = (char*)d_ws;
    bf16_t* xn = (bf16_t*)(ws);
    bf16_t* qkv = (bf16_t*)(ws + (size_t)4 * 1024 * 1024);
    bf16_t* o = (bf16_t*)(ws + (size_t)20 * 1024 * 1024);
    bf16_t* h = (bf16_t*)(ws + (size_t)24 * 1024 * 1024);
    bf16_t* hn = (bf16_t*)(ws + (size_t)28 * 1024 * 1024);
    bf16_t* act = (bf16_t*)(ws + (size_t)32 * 1024 * 1024);

    {
        const int shm = 3 * (128 + 192) * 128;
        (void)hipFuncSetAttribute((const void*)k_qkv, hipFuncAttributeMaxDynamicSharedMemorySize, shm);
        k_qkv<<<256, 512, shm, stream>>>(QKV_LNF ? x : xn, wqkv, wo, qkv);
    }
    k_attn<<<32 * (S_ / AT_QB), AT_NW * 64, 0, stream>>>(qkv, o);
    {
        const int shm = 6 * (128 + 64) * 128;
        (void)hipFuncSetAttribute((const void*)k_wo, hipFuncAttributeMaxDynamicSharedMemorySize, shm);
        k_wo<<<256, 512, shm, stream>>>(o, wo, x, h);
    }
    k_ln<<<S_ / 4, 256, 0, stream>>>(h, hn, (const unsigned*)w2, 0u, (const unsigned*)w2, 0u);
    {
        const int shm = 8 * 16384;
        (void)hipFuncSetAttribute((const void*)k_gu, hipFuncAttributeMaxDynamicSharedMemorySize, shm);
        k_gu<<<256, 512, shm, stream>>>(hn, w1, w3, act, (const unsigned*)w2);
    }
    {
        const int shm = 6 * (128 + 64) * 128;
        (void)hipFuncSetAttribute((const void*)k_down, hipFuncAttributeMaxDynamicSharedMemorySize, shm);
        k_down<<<256, 512, shm, stream>>>(act, w2, h, out);
    }
}
```

```cpp
#include <hip/hip_runtime.h>
#include <hip/hip_bf16.h>
#include <stdint.h>

typedef unsigned short bf16_t;
typedef short bf16x8 __attribute__((ext_vector_type(8)));
typedef short s16x4 __attribute__((ext_vector_type(4)));
typedef float f32x4 __attribute__((ext_vector_type(4)));
typedef unsigned u32x4 __attribute__((ext_vector_type(4)));
typedef unsigned u32x2 __attribute__((ext_vector_type(2)));
#define LAS __attribute__((address_space(3)))
#define DI __device__ __forceinline__

constexpr int S_ = 2048, D_ = 1024, F_ = 3584, H_ = 32, DH_ = 32, WIN_ = 256;

DI unsigned pk_bf16(float lo, float hi) {
    typedef __bf16 bf2 __attribute__((ext_vector_type(2)));
    bf2 v; v.x = (__bf16)lo; v.y = (__bf16)hi;
    return __builtin_bit_cast(unsigned, v);
}
template <int OFF> DI s16x4 tr_read_a(unsigned addr) { s16x4 r; asm volatile("ds_read_b64_tr_b16 %0, %1 offset:%2" : "=v"(r) : "v"(addr), "i"(OFF)); return r; }
DI void glds16(const void* g, LAS unsigned char* l) {
    __builtin_amdgcn_global_load_lds((const __attribute__((address_space(1))) unsigned*)g, (LAS unsigned*)l, 16, 0, 0);
}
#define WAIT_V(n) asm volatile("s_waitcnt vmcnt(" #n ")" ::: "memory")
#define WAIT_L(n) asm volatile("s_waitcnt lgkmcnt(" #n ")" ::: "memory")
#define BAR __builtin_amdgcn_s_barrier()
#define SCHED __builtin_amdgcn_sched_barrier(0)
#define STAMP_DECL(kid)
#define STAMP_AT()
#define STAMP_RT()
#define STAMP_RT2()

#define CODE_TOUCH(SYM, NB, ACC) do { unsigned lo_, hi_; \
        asm volatile("s_getpc_b64 s[20:21]\n\ts_add_u32 s20, s20, " #SYM "@rel32@lo+4\n\ts_addc_u32 s21, s21, " #SYM "@rel32@hi+12\n\ts_mov_b32 %0, s20\n\ts_mov_b32 %1, s21" \
                     : "=s"(lo_), "=s"(hi_) :: "s20", "s21", "scc"); \
        const unsigned o_ = (threadIdx.x & 63) * 128u; \
        if (o_ < (NB)) ACC += *(const __attribute__((address_space(1))) unsigned*)((((unsigned long long)hi_ << 32) | lo_) + o_); } while (0)
extern "C" __global__ __launch_bounds__(256) void k_ln(const bf16_t* __restrict__ in, bf16_t* __restrict__ out, const unsigned* __restrict__ pf, unsigned pf_lines,
                                                 const unsigned* __restrict__ pfb, unsigned pfb_lines) {
    const int row = blockIdx.x * 4 + (threadIdx.x >> 6), lane = threadIdx.x & 63;
    const u32x4* p = (const u32x4*)(in + (size_t)row * D_);
    u32x4 a = p[lane], b = p[lane + 64];
    unsigned pfv = 0;
    if (blockIdx.x < 8 && threadIdx.x < 64) { CODE_TOUCH(k_gu, 6400, pfv); CODE_TOUCH(k_down, 3328, pfv); }
    { const unsigned li = blockIdx.x * 256u + threadIdx.x; if (li < pf_lines) pfv += pf[(size_t)li * 32];
      else if (li - pf_lines < pfb_lines) pfv += pfb[(size_t)(li - pf_lines) * 32]; }
    float v[16];
#pragma unroll
    for (int i = 0; i < 4; ++i) { v[2 * i] = __uint_as_float(a[i] << 16); v[2 * i + 1] = __uint_as_float(a[i] & 0xffff0000u);
                                  v[8 + 2 * i] = __uint_as_float(b[i] << 16); v[8 + 2 * i + 1] = __uint_as_float(b[i] & 0xffff0000u); }
    float s = 0.f;
#pragma unroll
    for (int i = 0; i < 16; ++i) s += v[i];
#pragma unroll
    for (int o = 32; o >= 1; o >>= 1) s += __shfl_xor(s, o);
    const float mean = s * (1.0f / D_);
    float q = 0.f;
#pragma unroll
    for (int i = 0; i < 16; ++i) { v[i] -= mean; q += v[i] * v[i]; }
#pragma unroll
    for (int o = 32; o >= 1; o >>= 1) q += __shfl_xor(q, o);
    const float rstd = 1.0f / sqrtf(q * (1.0f / D_) + 1e-5f);
    u32x4 oa, ob;
#pragma unroll
    for (int i = 0; i < 4; ++i) { oa[i] = pk_bf16(v[2 * i] * rstd, v[2 * i + 1] * rstd); ob[i] = pk_bf16(v[8 + 2 * i] * rstd, v[8 + 2 * i + 1] * rstd); }
    u32x4* po = (u32x4*)(out + (size_t)row * D_);
    po[lane] = oa; po[lane + 64] = ob;
    asm volatile("" :: "v"(pfv));
}

DI int bx_of_k(int k) { return ((k >> 1) & 1) | (((k >> 3) & 1) << 1); }

enum { EPI_QKV = 0, EPI_RES = 1 };
#define QKV_LNF 1
DI u32x4 pair_swap(u32x2 t0, u32x2 t1) {
    auto a = __builtin_amdgcn_permlane16_swap(t0.x, t1.x, false, false);
    auto b = __builtin_amdgcn_permlane16_swap(t0.y, t1.y, false, false);
    return (u32x4){a[0], b[0], a[1], b[1]};
}

template <int N> DI void wait_v() { asm volatile("s_waitcnt vmcnt(%0)" :: "i"(N) : "memory"); }
template <int OFF> DI bf16x8 ld128_a(unsigned addr) { bf16x8 r; asm volatile("ds_read_b128 %0, %1 offset:%2" : "=v"(r) : "v"(addr), "i"(OFF)); return r; }

template <int BM, int BN, int WM, int WN, int NBUF, int EPI, int RM, int RN, int K>
DI void gemm_pp(const bf16_t* __restrict__ A, int lda, const bf16_t* __restrict__ W, int ldb,
                const bf16_t* __restrict__ res, bf16_t* __restrict__ out, int tiles_m) {
    extern __shared__ __attribute__((aligned(16))) unsigned char smem_raw[];
    LAS unsigned char* lds = (LAS unsigned char*)smem_raw;
    constexpr int MT = BM / WM / 16, NT = BN / WN / 16;
    constexpr int NPA = BM / 64, NPB = BN / 64, NP = NPA + NPB;
    constexpr int ABYTES = BM * 128, TILE_BYTES = (BM + BN) * 128;
    constexpr int nt = K / 64;
    constexpr bool LNF = (EPI == EPI_QKV) && QKV_LNF;
    static_assert(WM * WN == 8, "8 waves");
    static_assert(MT == 2 || MT == 4, "MT");
    static_assert(!LNF || (MT == 4 && WN == 4 && WM == 2), "LN fold geometry");
    STAMP_DECL(EPI == EPI_QKV ? 0 : (K == 1024 ? 1 : 2)); STAMP_RT(); STAMP_AT();
    const int tid = threadIdx.x, wid = __builtin_amdgcn_readfirstlane(tid >> 6), lane = tid & 63;
    const int wm = wid / WN, wn = wid % WN, fr = lane & 15, fq = lane >> 4;
    int tm, tn;
    { const int bid = blockIdx.x, xcd = bid & 7, idx = bid >> 3; const int regm = tiles_m / RM;
      const int rr = xcd % regm, rc = xcd / regm; tm = rr * RM + (idx % RM); tn = rc * RN + (idx / RM); }
    const int m0 = tm * BM, n0 = tn * BN;
    const bf16_t* asrc[NPA]; const bf16_t* bsrc[NPB];
#pragma unroll
    for (int i = 0; i < NPA; ++i) { const int row = (wid + 8 * i) * 8 + (lane >> 3), ch = (lane & 7) ^ ((row >> 1) & 7);
        asrc[i] = A + (size_t)(m0 + row) * lda + ch * 8; }
#pragma unroll
    for (int j = 0; j < NPB; ++j) { const int k = wid * 8 + (lane >> 3), s16 = lane & 7, n16 = (s16 >> 1) ^ bx_of_k(k);
        bsrc[j] = W + (size_t)k * ldb + n0 + j * 64 + n16 * 16 + (s16 & 1) * 8; }
    const size_t bstep = (size_t)64 * ldb;
    auto stage = [&](int t, int buf) {
        LAS unsigned char* base = lds + buf * TILE_BYTES + wid * 1024;
#pragma unroll
        for (int i = 0; i < NPA; ++i) glds16(asrc[i] + (size_t)t * 64, base + i * 8192);
#pragma unroll
        for (int j = 0; j < NPB; ++j) glds16(bsrc[j] + (size_t)t * bstep, base + ABYTES + j * 8192);
    };
    const int rotm = LNF ? wn : 0, krot = LNF ? wm : 0;
    const int sw = (fr >> 1) & 7;
    unsigned aadr[MT][2], badr[NT][2];
#pragma unroll
    for (int j = 0; j < MT; ++j)
#pragma unroll
        for (int sl = 0; sl < 2; ++sl) aadr[j][sl] = (unsigned)(size_t)lds + (wm * MT * 16 + ((j + rotm) & (MT - 1)) * 16 + fr) * 128 + (((((sl ^ krot) * 4) + fq) ^ sw) * 16);
    { const int q = (lane & 15) >> 2, p = lane & 3, xl = ((q >> 1) & 1) | ((fq & 1) << 1);
#pragma unroll
      for (int n = 0; n < NT; ++n) { const int ntg = wn * NT + n;
          const unsigned b0 = (unsigned)(size_t)lds + ABYTES + (ntg >> 2) * 8192 + (8 * fq + q) * 128 + (((ntg & 3) ^ xl) * 32) + p * 8;
          badr[n][0] = b0 + krot * 4096; badr[n][1] = b0 + (1 - krot) * 4096; } }
    f32x4 acc[MT][NT];
#pragma unroll
    for (int m = 0; m < MT; ++m)
#pragma unroll
        for (int n = 0; n < NT; ++n) acc[m][n] = (f32x4){0.f, 0.f, 0.f, 0.f};
    f32x4 rsacc = (f32x4){0.f, 0.f, 0.f, 0.f}, rqacc = rsacc, csacc[NT];
#pragma unroll
    for (int n = 0; n < NT; ++n) csacc[n] = rsacc;
    const bf16x8 ones = (bf16x8){0x3F80, 0x3F80, 0x3F80, 0x3F80, 0x3F80, 0x3F80, 0x3F80, 0x3F80};
    bf16x8 afA[MT][2], afB[MT][2]; s16x4 bA[NT][4], bB[NT][4];
#define P_READ(AF, BR, buf) do { const unsigned bo_ = (unsigned)((buf) * TILE_BYTES); \
        if constexpr (LNF) { \
            _Pragma("unroll") for (int n = 0; n < NT; ++n) { const unsigned b0_ = badr[n][0] + bo_, b1_ = badr[n][1] + bo_; \
                BR[n][0] = tr_read_a<0>(b0_); BR[n][1] = tr_read_a<512>(b0_); BR[n][2] = tr_read_a<0>(b1_); BR[n][3] = tr_read_a<512>(b1_); } \
            _Pragma("unroll") for (int j = 0; j < MT; ++j) { AF[j][0] = ld128_a<0>(aadr[j][0] + bo_); AF[j][1] = ld128_a<0>(aadr[j][1] + bo_); } \
        } else { \
            _Pragma("unroll") for (int n = 0; n < NT; ++n) { const unsigned ba = badr[n][0] + bo_; \
                BR[n][0] = tr_read_a<0>(ba); BR[n][1] = tr_read_a<512>(ba); BR[n][2] = tr_read_a<4096>(ba); BR[n][3] = tr_read_a<4608>(ba); } \
            const unsigned a0_ = aadr[0][0] + bo_, a1_ = aadr[0][1] + bo_; \
            AF[0][0] = ld128_a<0>(a0_); AF[0][1] = ld128_a<0>(a1_); AF[1][0] = ld128_a<2048>(a0_); AF[1][1] = ld128_a<2048>(a1_); \
            if constexpr (MT == 4) { AF[MT - 2][0] = ld128_a<4096>(a0_); AF[MT - 2][1] = ld128_a<4096>(a1_); AF[MT - 1][0] = ld128_a<6144>(a0_); AF[MT - 1][1] = ld128_a<6144>(a1_); } } } while (0)
#define P_WAITL(AF, BR) do { _Pragma("unroll") for (int n = 0; n < NT; ++n) \
        asm volatile("s_waitcnt lgkmcnt(0)" : "+v"(BR[n][0]), "+v"(BR[n][1]), "+v"(BR[n][2]), "+v"(BR[n][3]) :: "memory"); \
        _Pragma("unroll") for (int m = 0; m < MT; ++m) asm volatile("" : "+v"(AF[m][0]), "+v"(AF[m][1])); } while (0)
#define P_MMA(AF, BR) do { _Pragma("unroll") for (int kk = 0; kk < 2; ++kk) _Pragma("unroll") for (int m = 0; m < MT; ++m) _Pragma("unroll") for (int n = 0; n < NT; ++n) \
        acc[m][n] = __builtin_amdgcn_mfma_f32_16x16x32_bf16(__builtin_shufflevector(BR[n][2 * kk], BR[n][2 * kk + 1], 0, 1, 2, 3, 4, 5, 6, 7), AF[m][kk], acc[m][n], 0, 0, 0); \
        if constexpr (LNF) { _Pragma("unroll") for (int kk = 0; kk < 2; ++kk) { rsacc = __builtin_amdgcn_mfma_f32_16x16x32_bf16(ones, AF[0][kk], rsacc, 0, 0, 0); \
                rqacc = __builtin_amdgcn_mfma_f32_16x16x32_bf16(AF[0][kk], AF[0][kk], rqacc, 0, 0, 0); } \
            _Pragma("unroll") for (int n = 0; n < NT; ++n) csacc[n] = __builtin_amdgcn_mfma_f32_16x16x32_bf16(__builtin_shufflevector(BR[n][0], BR[n][1], 0, 1, 2, 3, 4, 5, 6, 7), ones, csacc[n], 0, 0, 0); } } while (0)
#define P_STEP(AFC, BRC, AFN, BRN, FULL, WN_, RESLD) do { \
        P_WAITL(AFC, BRC); \
        wait_v<WN_>(); \
        BAR; \
        const int nb_ = (buf + 1 == NBUF) ? 0 : buf + 1; \
        if (FULL || t + 1 < nt) P_READ(AFN, BRN, nb_); \
        if (FULL || t + NBUF < nt) stage(t + NBUF, buf); \
        if (RESLD) { _Pragma("unroll") for (int m = 0; m < MT; ++m) _Pragma("unroll") for (int n = 0; n < NT; ++n) \
            rres[m][n] = *(const u32x2*)(res + (size_t)(m0 + (wm * MT + m) * 16 + fr) * D_ + n0 + (wn * NT + n) * 16 + 4 * fq); } \
        P_MMA(AFC, BRC); \
        buf = nb_; ++t; } while (0)
    u32x2 rres[MT][NT];
    constexpr int NR = (EPI == EPI_RES) ? MT * NT : 0;
#pragma unroll
    for (int t = 0; t < NBUF; ++t) stage(t, t);
    wait_v<(NBUF - 1) * NP>();
    BAR;
    STAMP_AT();
    P_READ(afA, bA, 0);
    int buf = 0, t = 0;
    constexpr int nmain = ((nt - NBUF) / 2) * 2;
    for (; t < nmain;) {
        P_STEP(afA, bA, afB, bB, true, (NBUF - 2) * NP, false);
        P_STEP(afB, bB, afA, bA, true, (NBUF - 2) * NP, false);
    }
#define P_TAILW(tt) ((((tt) + NBUF - 1 < nt - 1 ? (tt) + NBUF - 1 : nt - 1) - ((tt) + 1) > 0 ? (((tt) + NBUF - 1 < nt - 1 ? (tt) + NBUF - 1 : nt - 1) - ((tt) + 1)) : 0) * NP + ((tt) > nt - NBUF ? NR : 0))
#define P_TAIL2(tt) do { t = (tt); P_STEP(afA, bA, afB, bB, false, P_TAILW(tt), (NR > 0 && (tt) == nt - NBUF)); \
        P_STEP(afB, bB, afA, bA, false, P_TAILW((tt) + 1), (NR > 0 && (tt) + 1 == nt - NBUF)); } while (0)
    if constexpr (nmain + 0 < nt) P_TAIL2(nmain + 0);
    if constexpr (nmain + 2 < nt) P_TAIL2(nmain + 2);
    if constexpr (nmain + 4 < nt) P_TAIL2(nmain + 4);
    if constexpr (nmain + 6 < nt) P_TAIL2(nmain + 6);
    static_assert(nmain + 8 >= nt, "tail");
#undef P_TAILW
#undef P_TAIL2
    STAMP_AT();
#undef P_READ
#undef P_WAITL
#undef P_MMA
#undef P_STEP
    if constexpr (EPI == EPI_RES) {
#pragma unroll
        for (int m = 0; m < MT; ++m)
#pragma unroll
            for (int n = 0; n < NT; ++n) asm volatile("" : "+v"(rres[m][n]));
    }
    typedef float f32x2 __attribute__((ext_vector_type(2)));
    LAS f32x2* stat = (LAS f32x2*)lds;
    LAS float* cpart = (LAS float*)(lds + BM * 8);
    if constexpr (LNF) {
        const float rq = (fr & 2) ? ((fr & 1) ? rqacc[3] : rqacc[2]) : ((fr & 1) ? rqacc[1] : rqacc[0]);
        const float mean = rsacc[0] * (1.0f / K), var = fmaxf(rq * (1.0f / K) - mean * mean, 0.f);
        if (fq == (fr >> 2)) stat[wm * 64 + wn * 16 + fr] = (f32x2){mean, 1.0f / sqrtf(var + 1e-5f)};
#pragma unroll
        for (int n = 0; n < NT; ++n) if (fr == 0) *(LAS f32x4*)(cpart + wm * BN + (wn * NT + n) * 16 + 4 * fq) = csacc[n];
        __syncthreads();
    }
    unsigned pfv = 0;
    if constexpr (EPI == EPI_QKV) { const unsigned li = blockIdx.x * 512u + threadIdx.x; if (li < (unsigned)((size_t)D_ * D_ * 2 / 128)) pfv = ((const unsigned*)res)[(size_t)li * 32];
        if (blockIdx.x >= 248 && wid == 0) { CODE_TOUCH(k_attn, 3456, pfv); CODE_TOUCH(k_wo, 4992, pfv); } }
    const int pcol = (fq & 1) ? 16 + 4 * (fq - 1) : 4 * fq;
    f32x4 csum[NT];
    if constexpr (LNF) {
#pragma unroll
        for (int n = 0; n < NT; ++n) csum[n] = *(const LAS f32x4*)(cpart + (wn * NT + n) * 16 + 4 * fq) + *(const LAS f32x4*)(cpart + BN + (wn * NT + n) * 16 + 4 * fq);
    }
#pragma unroll
    for (int m = 0; m < MT; ++m) {
        const int lrow = (wm * MT + ((m + rotm) & (MT - 1))) * 16 + fr, row = m0 + lrow;
        f32x2 st; if constexpr (LNF) st = stat[lrow];
        u32x2 w[NT];
#pragma unroll
        for (int n = 0; n < NT; ++n) {
            f32x4 v = acc[m][n];
            if constexpr (LNF) v = (v - csum[n] * st.x) * st.y;
            if constexpr (EPI == EPI_RES) { const u32x2 r = rres[m][n];
                v[0] += __uint_as_float(r.x << 16); v[1] += __uint_as_float(r.x & 0xffff0000u);
                v[2] += __uint_as_float(r.y << 16); v[3] += __uint_as_float(r.y & 0xffff0000u); }
            w[n].x = pk_bf16(v[0], v[1]); w[n].y = pk_bf16(v[2], v[3]);
        }
#pragma unroll
        for (int n = 0; n + 1 < NT; n += 2) {
            const u32x4 o = pair_swap(w[n], w[n + 1]);
            const int col = n0 + (wn * NT + n) * 16 + pcol;
            if constexpr (EPI == EPI_QKV) *(u32x4*)(out + ((size_t)(col >> 5) * S_ + row) * 32 + (col & 31)) = o;
            else *(u32x4*)(out + (size_t)row * D_ + col) = o;
        }
        if constexpr (NT & 1) {
            const int col = n0 + (wn * NT + NT - 1) * 16 + 4 * fq;
            if constexpr (EPI == EPI_QKV) *(u32x2*)(out + ((size_t)(col >> 5) * S_ + row) * 32 + (col & 31)) = w[NT - 1];
            else *(u32x2*)(out + (size_t)row * D_ + col) = w[NT - 1];
        }
    }
    asm volatile("" :: "v"(pfv));
    WAIT_V(0); STAMP_AT(); STAMP_RT2();
}

extern "C" __global__ __launch_bounds__(512, 2) void k_qkv(const bf16_t* __restrict__ A, const bf16_t* __restrict__ W, const bf16_t* __restrict__ wo, bf16_t* __restrict__ out) {
    gemm_pp<128, 192, 2, 4, 3, EPI_QKV, 8, 4, 1024>(A, D_, W, 3 * D_, wo, out, 16);
}
extern "C" __global__ __launch_bounds__(512, 2) void k_wo(const bf16_t* __restrict__ A, const bf16_t* __restrict__ W, const bf16_t* __restrict__ res, bf16_t* __restrict__ out) {
    gemm_pp<128, 64, 4, 2, 6, EPI_RES, 4, 8, 1024>(A, D_, W, D_, res, out, 16);
}
extern "C" __global__ __launch_bounds__(512, 2) void k_down(const bf16_t* __restrict__ A, const bf16_t* __restrict__ W, const bf16_t* __restrict__ res, bf16_t* __restrict__ out) {
    gemm_pp<128, 64, 4, 2, 6, EPI_RES, 4, 8, 3584>(A, F_, W, D_, res, out, 16);
}

extern "C" __global__ __launch_bounds__(512, 2) void k_gu(const bf16_t* __restrict__ A, const bf16_t* __restrict__ W1, const bf16_t* __restrict__ W3,
                                                       bf16_t* __restrict__ act, const unsigned* __restrict__ pf) {
    extern __shared__ __attribute__((aligned(16))) unsigned char smem_raw[];
    LAS unsigned char* lds = (LAS unsigned char*)smem_raw;
    constexpr int K = D_, HTB = 16384;
    STAMP_DECL(3); STAMP_RT(); STAMP_AT();
    const int tid = threadIdx.x, wid = __builtin_amdgcn_readfirstlane(tid >> 6), lane = tid & 63;
    const int wr = wid >> 2, wc = wid & 3, fr = lane & 15, fq = lane >> 4;
    if (blockIdx.x >= 224) {
        constexpr unsigned NL = (unsigned)((size_t)F_ * D_ * 2 / 128);
        const unsigned l0 = (blockIdx.x - 224) * 512u + threadIdx.x;
        unsigned a = 0;
#pragma unroll
        for (int i = 0; i < 4; ++i) { const unsigned li = l0 + i * 16384u; if (li < NL) a += ((const unsigned*)W1)[(size_t)li * 32] + ((const unsigned*)W3)[(size_t)li * 32]; }
#pragma unroll
        for (int i = 0; i < 4; ++i) { const unsigned li = l0 + i * 16384u; if (li < NL) a += pf[(size_t)li * 32]; }
        if (threadIdx.x < 64) CODE_TOUCH(k_down, 3328, a);
        asm volatile("" :: "v"(a));
        return;
    }
    int tm, tn;
    { const int bid = blockIdx.x, xcd = bid & 7, idx = bid >> 3; tm = (xcd & 1) * 4 + (idx & 3); tn = (xcd >> 1) * 7 + (idx >> 2); }
    const int m0 = tm * 256, f0 = tn * 128;
    constexpr int nt = K / 64;
    unsigned aoffs[2], boffs[2];
#pragma unroll
    for (int i = 0; i < 2; ++i) { const int row = (wid + 8 * i) * 8 + (lane >> 3), ch = (lane & 7) ^ ((row >> 1) & 7);
        aoffs[i] = (unsigned)(row * K + ch * 8) * 2u;
        const int k = wid * 8 + (lane >> 3), s16 = lane & 7, n16 = (s16 >> 1) ^ bx_of_k(k);
        boffs[i] = (unsigned)(k * F_ + i * 64 + n16 * 16 + (s16 & 1) * 8) * 2u; }
    const char* gA0 = (const char*)(A + (size_t)m0 * K); const char* gA1 = gA0 + (size_t)128 * K * 2;
    const char* gB0 = (const char*)(W1 + f0); const char* gB1 = (const char*)(W3 + f0);
    constexpr size_t akstep = 128, bkstep = (size_t)64 * F_ * 2;
    const unsigned ldsw = (unsigned)wid * 1024u;
#define G_SA(b, h) (((b) * 2 + (h)) * HTB)
#define G_SB(b, h) ((4 + (b) * 2 + (h)) * HTB)
#define G_STAGE(bufoff, gbase, voff) do { _Pragma("unroll") for (int _i = 0; _i < 2; ++_i) glds16((gbase) + (voff)[_i], lds + (bufoff) + ldsw + _i * 8192); } while (0)
    const int sw = (fr >> 1) & 7;
    const int aoff0 = (wr * 64 + fr) * 128 + (((wr * 4 + fq) ^ sw) * 16), aoff1 = aoff0 ^ 64;
    unsigned badr[2][2];
    { const int q = (lane & 15) >> 2, p = lane & 3, xl = ((q >> 1) & 1) | ((fq & 1) << 1);
#pragma unroll
      for (int n = 0; n < 2; ++n) { const int ntg = wc * 2 + n;
          const unsigned b0 = (unsigned)(size_t)lds + 4 * HTB + (ntg >> 2) * 8192 + (8 * fq + q) * 128 + (((ntg & 3) ^ xl) * 32) + p * 8;
          badr[n][0] = b0 + wr * 4096; badr[n][1] = b0 + (1 - wr) * 4096; } }
#define G_LDA(dst, b, h) do { _Pragma("unroll") for (int m = 0; m < 4; ++m) { dst[m][0] = *(const LAS bf16x8*)(lds + G_SA(b, h) + aoff0 + m * 2048); \
        dst[m][1] = *(const LAS bf16x8*)(lds + G_SA(b, h) + aoff1 + m * 2048); } } while (0)
#define G_LDB(dst, bo, h) do { _Pragma("unroll") for (int n = 0; n < 2; ++n) { const unsigned b0_ = badr[n][0] + (bo), b1_ = badr[n][1] + (bo); \
        dst[n][0] = tr_read_a<(h) * HTB>(b0_); dst[n][1] = tr_read_a<(h) * HTB + 512>(b0_); \
        dst[n][2] = tr_read_a<(h) * HTB>(b1_); dst[n][3] = tr_read_a<(h) * HTB + 512>(b1_); } } while (0)
#define G_WAITB(R) do { asm volatile("s_waitcnt lgkmcnt(0)" : "+v"(R[0][0]), "+v"(R[0][1]), "+v"(R[0][2]), "+v"(R[0][3]), "+v"(R[1][0]), "+v"(R[1][1]), "+v"(R[1][2]), "+v"(R[1][3]) :: "memory"); } while (0)
#define G_FR(R, n, k) __builtin_shufflevector(R[n][2 * (k)], R[n][2 * (k) + 1], 0, 1, 2, 3, 4, 5, 6, 7)
#define G_MMA(ai, bj, At, Bt) do { __builtin_amdgcn_s_setprio(1); _Pragma("unroll") for (int m = 0; m < 4; ++m) _Pragma("unroll") for (int n = 0; n < 2; ++n) \
        _Pragma("unroll") for (int k = 0; k < 2; ++k) acc[ai][bj][m][n] = __builtin_amdgcn_mfma_f32_16x16x32_bf16(G_FR(Bt, n, k), At[m][k], acc[ai][bj][m][n], 0, 0, 0); \
        __builtin_amdgcn_s_setprio(0); } while (0)
    f32x4 acc[2][2][4][2];
#pragma unroll
    for (int a = 0; a < 2; ++a)
#pragma unroll
        for (int b = 0; b < 2; ++b)
#pragma unroll
            for (int m = 0; m < 4; ++m)
#pragma unroll
                for (int n = 0; n < 2; ++n) acc[a][b][m][n] = (f32x4){0.f, 0.f, 0.f, 0.f};
    bf16x8 At[4][2]; s16x4 B0[2][4], B1[2][4];
    G_STAGE(G_SB(0, 0), gB0, boffs); G_STAGE(G_SA(0, 0), gA0, aoffs); G_STAGE(G_SB(0, 1), gB1, boffs); G_STAGE(G_SA(0, 1), gA1, aoffs);
    if (wr == 1) BAR;
    WAIT_V(4); BAR;
    G_STAGE(G_SB(1, 0), gB0 + bkstep, boffs); G_STAGE(G_SA(1, 0), gA0 + akstep, aoffs); G_STAGE(G_SB(1, 1), gB1 + bkstep, boffs);
    WAIT_V(6); BAR;
    STAMP_AT();
    for (int t = 0; t < nt; t += 2) {
        const int t1 = t + 1, t2 = (t + 2 < nt) ? t + 2 : nt - 1, t3 = (t + 3 < nt) ? t + 3 : nt - 1;
        G_LDB(B0, 0, 0); SCHED; G_LDA(At, 0, 0); G_STAGE(G_SA(1, 1), gA1 + t1 * akstep, aoffs);
        WAIT_L(8); BAR; G_WAITB(B0); G_MMA(0, 0, At, B0); BAR; SCHED;
        G_LDB(B1, 0, 1); G_STAGE(G_SB(0, 0), gB0 + t2 * bkstep, boffs);
        BAR; G_WAITB(B1); G_MMA(0, 1, At, B1); BAR;
        G_LDA(At, 0, 1); G_STAGE(G_SA(0, 0), gA0 + t2 * akstep, aoffs);
        BAR; WAIT_L(0); G_MMA(1, 0, At, B0); BAR; SCHED;
        G_STAGE(G_SB(0, 1), gB1 + t2 * bkstep, boffs);
        WAIT_V(6); BAR; G_MMA(1, 1, At, B1); BAR;
        G_LDB(B0, 2 * HTB, 0); SCHED; G_LDA(At, 1, 0); G_STAGE(G_SA(0, 1), gA1 + t2 * akstep, aoffs);
        WAIT_L(8); BAR; G_WAITB(B0); G_MMA(0, 0, At, B0); BAR; SCHED;
        G_LDB(B1, 2 * HTB, 1); G_STAGE(G_SB(1, 0), gB0 + t3 * bkstep, boffs);
        BAR; G_WAITB(B1); G_MMA(0, 1, At, B1); BAR;
        G_LDA(At, 1, 1); G_STAGE(G_SA(1, 0), gA0 + t3 * akstep, aoffs);
        BAR; WAIT_L(0); G_MMA(1, 0, At, B0); BAR; SCHED;
        G_STAGE(G_SB(1, 1), gB1 + t3 * bkstep, boffs);
        WAIT_V(6); BAR; G_MMA(1, 1, At, B1); BAR;
    }
    STAMP_AT();
    WAIT_V(0);
    if (wr == 0) BAR;
    const int pcol = (fq & 1) ? 16 + 4 * (fq - 1) : 4 * fq;
#pragma unroll
    for (int ai = 0; ai < 2; ++ai)
#pragma unroll
        for (int m = 0; m < 4; ++m) {
            const int lrow = ai * 128 + wr * 64 + m * 16 + fr, row = m0 + lrow;
            u32x2 w[2];
#pragma unroll
            for (int n = 0; n < 2; ++n) {
                const f32x4 g = acc[ai][0][m][n], u = acc[ai][1][m][n];
                float o[4];
#pragma unroll
                for (int j = 0; j < 4; ++j) o[j] = g[j] * __builtin_amdgcn_rcpf(1.0f + __builtin_amdgcn_exp2f(g[j] * -1.4426950408889634f)) * u[j];
                w[n].x = pk_bf16(o[0], o[1]); w[n].y = pk_bf16(o[2], o[3]);
            }
            *(u32x4*)(act + (size_t)row * F_ + f0 + wc * 32 + pcol) = pair_swap(w[0], w[1]);
        }
    WAIT_V(0); STAMP_AT(); STAMP_RT2();
#undef G_SA
#undef G_SB
#undef G_STAGE
#undef G_LDA
#undef G_LDB
#undef G_MMA
#undef G_WAITB
#undef G_FR
}

constexpr int AT_QB = 256, AT_ROWS = AT_QB + 256, AT_NW = AT_QB / 16;
extern "C" __global__ __launch_bounds__(AT_NW * 64, 4) void k_attn(const bf16_t* __restrict__ qkv, bf16_t* __restrict__ o) {
    __shared__ __attribute__((aligned(16))) unsigned char sm[2 * AT_ROWS * 64];
    LAS unsigned char* lk = (LAS unsigned char*)sm; LAS unsigned char* lv = lk + AT_ROWS * 64;
    const int tid = threadIdx.x, wid = __builtin_amdgcn_readfirstlane(tid >> 6), lane = tid & 63, fr = lane & 15, fq = lane >> 4;
    const int head = blockIdx.x & 31, qb = blockIdx.x >> 5, q0 = qb * AT_QB;
    const bf16_t* Q = qkv + (size_t)head * S_ * 32;
    const bf16_t* Kg = qkv + (size_t)(32 + head) * S_ * 32;
    const bf16_t* Vg = qkv + (size_t)(64 + head) * S_ * 32;
#pragma unroll
    for (int pc = wid; pc < AT_ROWS / 16; pc += AT_NW) {
        const int row = pc * 16 + (lane >> 2), slot = lane & 3;
        int key = q0 - 256 + row; key = key < 0 ? 0 : (key > S_ - 1 ? S_ - 1 : key);
        const int chk = slot ^ ((-(row >> 2)) & 3), chv = slot ^ (((row >> 2) & 1) << 1);
        glds16(Kg + (size_t)key * 32 + chk * 8, lk + pc * 1024);
        glds16(Vg + (size_t)key * 32 + chv * 8, lv + pc * 1024);
    }
    const int qrow = q0 + wid * 16 + fr;
    bf16x8 qf;
    asm volatile("global_load_dwordx4 %0, %1, off" : "=v"(qf) : "v"(Q + (size_t)qrow * 32 + fq * 8));
    asm volatile("s_waitcnt vmcnt(0)" : "+v"(qf) :: "memory");
    __builtin_amdgcn_s_barrier();
    unsigned ctv = 0;
    if (blockIdx.x < 8 && wid == 0) { CODE_TOUCH(k_wo, 4992, ctv); CODE_TOUCH(k_ln, 1152, ctv); CODE_TOUCH(k_gu, 6400, ctv); }
    const int kbase = q0 + wid * 16 - 256;
    const int istart = kbase < 0 ? (-kbase) >> 4 : 0;
    f32x4 s[17];
    const int koff = (wid * 16 + fr) * 64 + ((fq ^ ((-(fr >> 2)) & 3)) * 16);
#pragma unroll
    for (int i = 0; i < 17; ++i) {
        const bf16x8 kf = *(const LAS bf16x8*)(lk + koff + i * 1024);
        s[i] = __builtin_amdgcn_mfma_f32_16x16x32_bf16(kf, qf, (f32x4){0.f, 0.f, 0.f, 0.f}, 0, 0, 0);
    }
    asm volatile("s_nop 7\n\ts_nop 7\n\ts_nop 3" : "+v"(s[0]), "+v"(s[1]), "+v"(s[2]), "+v"(s[3]), "+v"(s[4]), "+v"(s[5]), "+v"(s[6]), "+v"(s[7]), "+v"(s[8]),
                 "+v"(s[9]), "+v"(s[10]), "+v"(s[11]), "+v"(s[12]), "+v"(s[13]), "+v"(s[14]), "+v"(s[15]), "+v"(s[16]));
    const float NEG = -3.0e38f;
#pragma unroll
    for (int r = 0; r < 4; ++r) { if (!(4 * fq + r > fr)) s[0][r] = NEG; if (!(4 * fq + r <= fr)) s[16][r] = NEG; }
    if (istart > 0) {
#pragma unroll
        for (int i = 0; i < 16; ++i) if (i < istart) s[i] = (f32x4){NEG, NEG, NEG, NEG};
    }
    float mx = NEG;
#pragma unroll
    for (int i = 0; i < 17; ++i) { asm("v_max3_f32 %0, %1, %2, %3" : "=v"(mx) : "v"(mx), "v"(s[i][0]), "v"(s[i][1]));
                                   asm("v_max3_f32 %0, %1, %2, %3" : "=v"(mx) : "v"(mx), "v"(s[i][2]), "v"(s[i][3])); }
    mx = fmaxf(mx, __shfl_xor(mx, 16)); mx = fmaxf(mx, __shfl_xor(mx, 32));
    const float c = 0.17677669529663687f * 1.4426950408889634f;
    const float mc = -mx * c;
    typedef float f32x2 __attribute__((ext_vector_type(2)));
    const f32x2 c2 = {c, c}, mc2 = {mc, mc};
#pragma unroll
    for (int i = 0; i < 17; ++i) {
        const f32x2 lo = __builtin_elementwise_fma((f32x2){s[i][0], s[i][1]}, c2, mc2), hi = __builtin_elementwise_fma((f32x2){s[i][2], s[i][3]}, c2, mc2);
        s[i][0] = __builtin_amdgcn_exp2f(lo.x); s[i][1] = __builtin_amdgcn_exp2f(lo.y); s[i][2] = __builtin_amdgcn_exp2f(hi.x); s[i][3] = __builtin_amdgcn_exp2f(hi.y);
    }
    f32x4 oacc[2] = {(f32x4){0.f, 0.f, 0.f, 0.f}, (f32x4){0.f, 0.f, 0.f, 0.f}};
    f32x4 osum = (f32x4){0.f, 0.f, 0.f, 0.f};
    const bf16x8 ones = (bf16x8){0x3F80, 0x3F80, 0x3F80, 0x3F80, 0x3F80, 0x3F80, 0x3F80, 0x3F80};
    const int q4 = (lane & 15) >> 2, p4 = lane & 3;
    const unsigned va0 = (unsigned)(size_t)lv + (wid * 16 + 4 * fq + q4) * 64 + p4 * 8 + ((fq & 1) << 5), va1 = va0 ^ 32;
    s16x4 vr[2][4];
#define V_READ(SET, cc) do { vr[SET][0] = tr_read_a<(cc) * 2048>(va0); vr[SET][1] = tr_read_a<(cc) * 2048 + ((cc) < 8 ? 1024 : 0)>(va0); \
        vr[SET][2] = tr_read_a<(cc) * 2048>(va1); vr[SET][3] = tr_read_a<(cc) * 2048 + ((cc) < 8 ? 1024 : 0)>(va1); } while (0)
#define V_STEP(cc) do { constexpr int cur = (cc) & 1; \
        if constexpr ((cc) < 8) { V_READ(cur ^ 1, (cc) + 1); asm volatile("s_waitcnt lgkmcnt(4)" : "+v"(vr[cur][0]), "+v"(vr[cur][1]), "+v"(vr[cur][2]), "+v"(vr[cur][3])); } \
        else asm volatile("s_waitcnt lgkmcnt(0)" : "+v"(vr[cur][0]), "+v"(vr[cur][1]), "+v"(vr[cur][2]), "+v"(vr[cur][3])); \
        u32x4 pw; pw.x = pk_bf16(s[2 * (cc)][0], s[2 * (cc)][1]); pw.y = pk_bf16(s[2 * (cc)][2], s[2 * (cc)][3]); \
        if constexpr ((cc) < 8) { pw.z = pk_bf16(s[2 * (cc) + 1][0], s[2 * (cc) + 1][1]); pw.w = pk_bf16(s[2 * (cc) + 1][2], s[2 * (cc) + 1][3]); } else { pw.z = 0u; pw.w = 0u; } \
        const bf16x8 pf = __builtin_bit_cast(bf16x8, pw); \
        osum = __builtin_amdgcn_mfma_f32_16x16x32_bf16(ones, pf, osum, 0, 0, 0); \
        oacc[0] = __builtin_amdgcn_mfma_f32_16x16x32_bf16(__builtin_shufflevector(vr[cur][0], vr[cur][1], 0, 1, 2, 3, 4, 5, 6, 7), pf, oacc[0], 0, 0, 0); \
        oacc[1] = __builtin_amdgcn_mfma_f32_16x16x32_bf16(__builtin_shufflevector(vr[cur][2], vr[cur][3], 0, 1, 2, 3, 4, 5, 6, 7), pf, oacc[1], 0, 0, 0); } while (0)
    V_READ(0, 0);
    V_STEP(0); V_STEP(1); V_STEP(2); V_STEP(3); V_STEP(4); V_STEP(5); V_STEP(6); V_STEP(7); V_STEP(8);
#undef V_READ
#undef V_STEP
    const float inv = 1.0f / osum[0];
#pragma unroll
    for (int dt = 0; dt < 2; ++dt) {
        u32x2 w; w.x = pk_bf16(oacc[dt][0] * inv, oacc[dt][1] * inv); w.y = pk_bf16(oacc[dt][2] * inv, oacc[dt][3] * inv);
        *(u32x2*)(o + (size_t)qrow * D_ + head * 32 + dt * 16 + 4 * fq) = w;
    }
    asm volatile("" :: "v"(ctv));
}

extern "C" void kernel_launch(void* const* d_in, const int* in_sizes, int n_in, void* d_out, int out_size, void* d_ws, size_t ws_size,
                              hipStream_t stream) {
    const bf16_t* x = (const bf16_t*)d_in[0];
    const bf16_t* wqkv = (const bf16_t*)d_in[1];
    const bf16_t* wo = (const bf16_t*)d_in[2];
    const bf16_t* w1 = (const bf16_t*)d_in[4];
    const bf16_t* w3 = (const bf16_t*)d_in[5];
    const bf16_t* w2 = (const bf16_t*)d_in[6];
    bf16_t* out = (bf16_t*)d_out;
    char* ws = (char*)d_ws;
    bf16_t* xn = (bf16_t*)(ws);
    bf16_t* qkv = (bf16_t*)(ws + (size_t)4 * 1024 * 1024);
    bf16_t* o = (bf16_t*)(ws + (size_t)20 * 1024 * 1024);
    bf16_t* h = (bf16_t*)(ws + (size_t)24 * 1024 * 1024);
    bf16_t* hn = (bf16_t*)(ws + (size_t)28 * 1024 * 1024);
    bf16_t* act = (bf16_t*)(ws + (size_t)32 * 1024 * 1024);

    {
        const int shm = 3 * (128 + 192) * 128;
        (void)hipFuncSetAttribute((const void*)k_qkv, hipFuncAttributeMaxDynamicSharedMemorySize, shm);
        k_qkv<<<256, 512, shm, stream>>>(QKV_LNF ? x : xn, wqkv, wo, qkv);
    }
    k_attn<<<32 * (S_ / AT_QB), AT_NW * 64, 0, stream>>>(qkv, o);
    {
        const int shm = 6 * (128 + 64) * 128;
        (void)hipFuncSetAttribute((const void*)k_wo, hipFuncAttributeMaxDynamicSharedMemorySize, shm);
        k_wo<<<256, 512, shm, stream>>>(o, wo, x, h);
    }
    k_ln<<<S_ / 4, 256, 0, stream>>>(h, hn, (const unsigned*)w2, 0u, (const unsigned*)w2, 0u);
    {
        const int shm = 8 * 16384;
        (void)hipFuncSetAttribute((const void*)k_gu, hipFuncAttributeMaxDynamicSharedMemorySize, shm);
        k_gu<<<256, 512, shm, stream>>>(hn, w1, w3, act, (const unsigned*)w2);
    }
    {
        const int shm = 6 * (128 + 64) * 128;
        (void)hipFuncSetAttribute((const void*)k_down, hipFuncAttributeMaxDynamicSharedMemorySize, shm);
        k_down<<<256, 512, shm, stream>>>(act, w2, h, out);
    }
}
```
